```python
import torch

def _softmax(x, axis=-1):
    return torch.softmax(x, dim=axis)
def _gelu(x):
    return torch.nn.functional.gelu(x, approximate="tanh")
def _silu(x):
    return torch.nn.functional.silu(x)
def _sigmoid(x):
    return torch.sigmoid(x)
def _layernorm(x, axis=-1, eps=1e-5):
    m = x.mean(dim=axis, keepdim=True)
    v = x.var(dim=axis, keepdim=True, unbiased=False)
    return (x - m) / torch.sqrt(v + eps)
def _scatter_add(tgt, idx, src, axis=0):
    return tgt.clone().index_add_(axis, idx, src)
def _segment_sum(src, seg, n):
    out = torch.zeros((n,) + src.shape[1:], dtype=src.dtype)
    return out.index_add_(0, seg, src)
def _pad(x, pw):
    flat = tuple(v for pair in reversed(pw) for v in pair)
    return torch.nn.functional.pad(x, flat)

kernel_name = 'standalone__gqa_softmax__near_ridge'

def setup_inputs():

    _w = (lambda a: (lambda t: t.to(torch.bfloat16) if t.is_floating_point() else t)(torch.as_tensor(a)))
    B, Hq, Hkv, Sq, Sk, D = 8, 32, 4, 32, 2048, 128
    return {
        "q": _w((lambda *s: torch.randn(*s, dtype=torch.bfloat16) * 0.8)(B, Hq, Sq, D)),
        "k": _w((lambda *s: torch.randn(*s, dtype=torch.bfloat16) * 0.8)(B, Hkv, Sk, D)),
        "v": _w((lambda *s: torch.randn(*s, dtype=torch.bfloat16) * 0.05)(B, Hkv, Sk, D)),
    }

def reference(q, k, v):
    B, Hq, Sq, D = q.shape
    Hkv = k.shape[1]
    rep = Hq // Hkv
    k = (lambda x, n, axis: torch.repeat_interleave(x, n, dim=axis))(k, rep, axis=1)
    v = (lambda x, n, axis: torch.repeat_interleave(x, n, dim=axis))(v, rep, axis=1)
    scale = D ** -0.5
    scores = torch.einsum("bhqd,bhkd->bhqk", q, k) * scale
    p = _softmax(scores, axis=-1)
    return torch.einsum("bhqk,bhkd->bhqd", p, v)
```

```cpp
#include <hip/hip_runtime.h>
#include <hip/hip_bf16.h>
#include <cstdio>

#define ATTN_B 8
#define ATTN_HQ 32
#define ATTN_HKV 4
#define ATTN_SQ 32
#define ATTN_SK 2048
#define ATTN_NSPLIT 8
#define ATTN_THR 8
#define ATTN_KVCONV 0
#define ATTN_CAUSAL 0
#define ATTN_LENS 0
#define ATTN_QPARK (4 + ATTN_LENS)
#define ATTN_WG4 1
#define ATTN_QPARK4 4
#define ATTN_XCHG4 1
constexpr int B = ATTN_B, HQ = ATTN_HQ, HKV = ATTN_HKV, SQ = ATTN_SQ, SK = ATTN_SK;
constexpr int NSPLIT = ATTN_NSPLIT;
constexpr float THR = ATTN_THR;
constexpr int CAUSAL = ATTN_CAUSAL;
constexpr int HAS_LENS = ATTN_LENS;
constexpr int KV_CONVERT = ATTN_KVCONV;
constexpr int QPARK = ATTN_QPARK;
constexpr int WG4 = ATTN_WG4;
constexpr int XCHG4 = ATTN_XCHG4;
constexpr int QPARK4 = ATTN_QPARK4;
constexpr int D = 128, NW = 8, QBLK = 32, KVBLK = 64;
constexpr float SCALE = 0.088388347648318447f;
constexpr int G = HQ / HKV, ROWS = G * SQ, NT = SK / KVBLK, TPS = NT / NSPLIT;
constexpr int NWB = (WG4 && ROWS <= QBLK * 4 && !CAUSAL && !HAS_LENS && TPS % 4 == 0) ? 4 : NW, NSB = NSPLIT * (NW / NWB), TPSB = NT / NSB;
static_assert(HQ % HKV == 0 && ROWS >= 1 && ROWS <= QBLK * NW, "G*SQ must fit the workgroup's 256 MFMA rows (larger SQ -> dense body)");
static_assert(!(CAUSAL && HAS_LENS && !KV_CONVERT), "CAUSAL + HAS_LENS on the f32 convert-on-load loop spills 2 VGPRs (ROCm 7.2, QPARK 4..8): set KV_CONVERT 1 (bf16 loop, 0 spill) for that combination");
static_assert(SK % KVBLK == 0 && NT % NSPLIT == 0 && TPS >= 2 && TPS % 2 == 0, "SK must give every split an even number (>= 2) of 64-key tiles");
static_assert(NT % NSB == 0 && TPSB >= 2 && TPSB % 2 == 0, "internal: half-split tile count (NWB falls back to 8 waves when TPS % 4 != 0)");
constexpr float L2E = 1.4426950408889634f, SL2E = SCALE * L2E, THR2 = THR * L2E;
constexpr size_t SHM_V = KVBLK * D * 2, SHM_K = KVBLK * D * 2, SHM_WS = 2 * SHM_V + 2 * SHM_K;
template <int NWT> constexpr size_t shm_qp() { return SHM_WS + ((NWT == NW || !XCHG4) ? (size_t)NWT * 64 * 4 : 0); }
static_assert(QPARK >= 0 && QPARK <= 8 && QPARK4 >= 0 && QPARK4 <= 8, "QPARK");
constexpr size_t WS_PML = 4096, PML_BYTES = (size_t)B * HQ * SQ * NSB * 2 * 4;
constexpr size_t WS_PO = (WS_PML + PML_BYTES + 255) / 256 * 256, WS_PEND = NSB > 1 ? WS_PO + (size_t)B * HQ * SQ * NSB * D * 4 : WS_PML;
constexpr size_t NQ = (size_t)B * HQ * SQ * D, NKV = (size_t)B * HKV * SK * D;
constexpr size_t WS_QB = (WS_PEND + 255) / 256 * 256, WS_KB = WS_QB + NQ * 2, WS_VB = WS_KB + NKV * 2;
constexpr size_t WS_END = KV_CONVERT ? WS_VB + NKV * 2 : WS_PEND;

using bf16 = __hip_bfloat16;
using bf16x8 = __attribute__((ext_vector_type(8))) short;
using s16x4 = __attribute__((ext_vector_type(4))) short;
using f32x16 = __attribute__((ext_vector_type(16))) float;
using f32x4 = __attribute__((ext_vector_type(4))) float;
using u32x4 = __attribute__((ext_vector_type(4))) unsigned;
template <class T> struct InCfg;
template <> struct InCfg<__hip_bfloat16> { static constexpr int NWT = NWB, QP = NWB == 4 ? QPARK4 : 0; };
template <> struct InCfg<float> { static constexpr int NWT = NW, QP = QPARK; };
template <class T> constexpr int nw_for() { return InCfg<T>::NWT; }
template <class T> constexpr int ns_for() { return NSPLIT * (NW / InCfg<T>::NWT); }
template <class T> constexpr size_t shm_bytes() { return shm_qp<InCfg<T>::NWT>() + (size_t)InCfg<T>::QP * 64 * InCfg<T>::NWT * 16; }
#define KSWZ(row, colB) ((row) * 256 + ((colB) ^ (((row) & 7) << 4)))
#define SBAR() __builtin_amdgcn_sched_barrier(0)
__device__ __forceinline__ int crow(int r, int hi) { return (r & 3) + 8 * (r >> 2) + 4 * hi; }
__device__ __forceinline__ unsigned cvtpk(float lo, float hi) { unsigned r; asm volatile("v_cvt_pk_bf16_f32 %0, %1, %2" : "=v"(r) : "v"(lo), "v"(hi)); return r; }
__device__ __forceinline__ bf16x8 pack8(f32x4 a, f32x4 b) { u32x4 w = {cvtpk(a[0], a[1]), cvtpk(a[2], a[3]), cvtpk(b[0], b[1]), cvtpk(b[2], b[3])}; return *reinterpret_cast<bf16x8*>(&w); }
__device__ __forceinline__ bf16x8 ld8(const bf16* p) { return *reinterpret_cast<const bf16x8*>(p); }
__device__ __forceinline__ bf16x8 ld8(const float* p) { return pack8(*reinterpret_cast<const f32x4*>(p), *reinterpret_cast<const f32x4*>(p + 4)); }
template <class T, int NR> struct Stage;
template <> struct Stage<bf16, 2> { bf16x8 v0, v1, k0, k1;
  __device__ __forceinline__ void load(const bf16* V0, const bf16* V1, const bf16* K0, const bf16* K1) { v0 = ld8(V0); v1 = ld8(V1); k0 = ld8(K0); k1 = ld8(K1); }
  __device__ __forceinline__ bf16x8 gv0() const { return v0; } __device__ __forceinline__ bf16x8 gv1() const { return v1; }
  __device__ __forceinline__ bf16x8 gk0() const { return k0; } __device__ __forceinline__ bf16x8 gk1() const { return k1; } };
template <> struct Stage<float, 2> { f32x4 v0a, v0b, v1a, v1b, k0a, k0b, k1a, k1b;
  __device__ __forceinline__ void load(const float* V0, const float* V1, const float* K0, const float* K1) {
    v0a = *(const f32x4*)V0; v0b = *(const f32x4*)(V0 + 4); v1a = *(const f32x4*)V1; v1b = *(const f32x4*)(V1 + 4);
    k0a = *(const f32x4*)K0; k0b = *(const f32x4*)(K0 + 4); k1a = *(const f32x4*)K1; k1b = *(const f32x4*)(K1 + 4); }
  __device__ __forceinline__ bf16x8 gv0() const { return pack8(v0a, v0b); } __device__ __forceinline__ bf16x8 gv1() const { return pack8(v1a, v1b); }
  __device__ __forceinline__ bf16x8 gk0() const { return pack8(k0a, k0b); } __device__ __forceinline__ bf16x8 gk1() const { return pack8(k1a, k1b); } };
template <int NR> struct Stage<bf16, NR> { bf16x8 v[NR], k[NR];
  __device__ __forceinline__ void ldv(int i, const bf16* p) { v[i] = ld8(p); } __device__ __forceinline__ void ldk(int i, const bf16* p) { k[i] = ld8(p); }
  __device__ __forceinline__ bf16x8 gv(int i) const { return v[i]; } __device__ __forceinline__ bf16x8 gk(int i) const { return k[i]; } };
template <int NR> struct Stage<float, NR> { f32x4 va[NR], vb[NR], ka[NR], kb[NR];
  __device__ __forceinline__ void ldv(int i, const float* p) { va[i] = *(const f32x4*)p; vb[i] = *(const f32x4*)(p + 4); }
  __device__ __forceinline__ void ldk(int i, const float* p) { ka[i] = *(const f32x4*)p; kb[i] = *(const f32x4*)(p + 4); }
  __device__ __forceinline__ bf16x8 gv(int i) const { return pack8(va[i], vb[i]); } __device__ __forceinline__ bf16x8 gk(int i) const { return pack8(ka[i], kb[i]); } };
__device__ __forceinline__ int voff(int row, int sc) { return (row >> 5) * 8192 + 2 * ((sc >> 4) * 512 + (row & 31) * 16 + (sc & 8)); }

__device__ __forceinline__ void partialSM(f32x16& p0, f32x16& p1, float& m_reg, float& msub, float& alpha, int kbase, int lim, int lim0) {
  if constexpr (CAUSAL || HAS_LENS) { if (kbase + KVBLK - 1 > lim0) {
    const float ninf = -__builtin_inff(); const int hi = (threadIdx.x & 63) >> 5;
    for (int r = 0; r < 16; ++r) { const int k = kbase + crow(r, hi); p0[r] = k > lim ? ninf : p0[r]; p1[r] = k + 32 > lim ? ninf : p1[r]; } } }
  float pmax = p0[0]; for (int r = 1; r < 16; ++r) pmax = fmaxf(pmax, p0[r]); for (int r = 0; r < 16; ++r) pmax = fmaxf(pmax, p1[r]);
  { auto rr = __builtin_amdgcn_permlane32_swap(__float_as_uint(pmax), __float_as_uint(pmax), false, false);
    pmax = fmaxf(__uint_as_float(rr[0]), __uint_as_float(rr[1])); }
  pmax *= SL2E;
  if (__builtin_expect(__all(pmax - m_reg <= THR2), 1)) { alpha = 1.f; }
  else { float mn = fmaxf(m_reg, pmax); msub = (mn == -__builtin_inff()) ? 0.f : mn; alpha = __builtin_amdgcn_exp2f(m_reg - msub); m_reg = mn; }
  float nm = -msub;
  for (int r = 0; r < 16; ++r) p0[r] = fmaf(p0[r], SL2E, nm); for (int r = 0; r < 16; ++r) p1[r] = fmaf(p1[r], SL2E, nm);
  for (int r = 0; r < 16; ++r) p0[r] = __builtin_amdgcn_exp2f(p0[r]);
}
__device__ __forceinline__ void finishSM(f32x16& p0, f32x16& p1, float alpha, float& l_reg, bf16x8& pa0, bf16x8& pa1, bf16x8& pa2, bf16x8& pa3) {
  for (int r = 0; r < 16; ++r) p1[r] = __builtin_amdgcn_exp2f(p1[r]);
  float ps = 0; for (int r = 0; r < 16; ++r) ps += p0[r]; for (int r = 0; r < 16; ++r) ps += p1[r];
  { auto rr = __builtin_amdgcn_permlane32_swap(__float_as_uint(ps), __float_as_uint(ps), false, false); ps = __uint_as_float(rr[0]) + __uint_as_float(rr[1]); }
  l_reg = l_reg * alpha + ps;
#define PK4(P, BASE, OUT) do { unsigned a0 = cvtpk(P[BASE + 0], P[BASE + 1]), a1 = cvtpk(P[BASE + 2], P[BASE + 3]); \
    unsigned b0 = cvtpk(P[BASE + 4], P[BASE + 5]), b1 = cvtpk(P[BASE + 6], P[BASE + 7]); \
    auto r0 = __builtin_amdgcn_permlane32_swap(a0, b0, false, false); auto r1 = __builtin_amdgcn_permlane32_swap(a1, b1, false, false); \
    u32x4 w = {r0[0], r1[0], r0[1], r1[1]}; OUT = *reinterpret_cast<bf16x8*>(&w); } while (0)
  PK4(p0, 0, pa0); PK4(p0, 8, pa1); PK4(p1, 0, pa2); PK4(p1, 8, pa3);
#undef PK4
}
template <int QP, int NWT> __device__ __forceinline__ void qkt(f32x16& p0, f32x16& p1, const bf16* Ks, const bf16x8* qr, const char* qp, int r32, int hi) {
  p0 = f32x16{}; p1 = f32x16{};
#pragma unroll
  for (int d0 = 0; d0 < 8; ++d0) { int cb = (d0 * 16 + hi * 8) * 2;
    bf16x8 b0 = *reinterpret_cast<const bf16x8*>((const char*)Ks + KSWZ(r32, cb));
    bf16x8 b1 = *reinterpret_cast<const bf16x8*>((const char*)Ks + KSWZ(32 + r32, cb));
    const bf16x8 q = d0 < 8 - QP ? qr[d0] : *reinterpret_cast<const bf16x8*>(qp + (d0 - (8 - QP)) * (64 * NWT * 16));
    p0 = __builtin_amdgcn_mfma_f32_32x32x16_bf16(b0, q, p0, 0, 0, 0);
    p1 = __builtin_amdgcn_mfma_f32_32x32x16_bf16(b1, q, p1, 0, 0, 0); }
}
__device__ __forceinline__ void pv_d0(f32x16* o, int vb, bf16x8 pa0, bf16x8 pa1, bf16x8 pa2, bf16x8 pa3) {
#pragma unroll
  for (int d0 = 0; d0 < 4; ++d0) { s16x4 lo[4], hi[4];
#pragma unroll
    for (int ks = 0; ks < 4; ++ks) { int a = vb + d0 * 2048 + (ks >> 1) * 8192 + (ks & 1) * 512;
      asm volatile("ds_read_b64_tr_b16 %0,%1" : "=&v"(lo[ks]) : "v"(a) : "memory");
      asm volatile("ds_read_b64_tr_b16 %0,%1 offset:128" : "=&v"(hi[ks]) : "v"(a) : "memory"); }
    asm volatile("s_waitcnt lgkmcnt(0)" ::: "memory"); SBAR();
#define PK(k) (bf16x8){lo[k][0], lo[k][1], lo[k][2], lo[k][3], hi[k][0], hi[k][1], hi[k][2], hi[k][3]}
    o[d0] = __builtin_amdgcn_mfma_f32_32x32x16_bf16(pa0, PK(0), o[d0], 0, 0, 0);
    o[d0] = __builtin_amdgcn_mfma_f32_32x32x16_bf16(pa1, PK(1), o[d0], 0, 0, 0);
    o[d0] = __builtin_amdgcn_mfma_f32_32x32x16_bf16(pa2, PK(2), o[d0], 0, 0, 0);
    o[d0] = __builtin_amdgcn_mfma_f32_32x32x16_bf16(pa3, PK(3), o[d0], 0, 0, 0);
#undef PK
  }
}

template <class TQ, class TKV>
__global__ __launch_bounds__(64 * InCfg<TKV>::NWT, NW / InCfg<TKV>::NWT)
void gqa_chunk_fwd(const TQ* __restrict__ Q, const TKV* __restrict__ K, const TKV* __restrict__ V, const int* __restrict__ lens, float* __restrict__ O, float* __restrict__ pml, float* __restrict__ po) {
  constexpr int NWT = InCfg<TKV>::NWT, NS = NSPLIT * (NW / NWT), TPS = NT / NS;
  constexpr int NR = KVBLK / (4 * NWT);
  static_assert(ROWS <= QBLK * NWT, "rows must fit this form's waves");
  const int tid = threadIdx.x, wid = tid >> 6, lane = tid & 63, r32 = lane & 31, hi = lane >> 5;
  const int split = blockIdx.x, kvh = blockIdx.y, b = blockIdx.z;
  const TQ* Qg = Q + ((size_t)b * HQ + (size_t)kvh * G) * SQ * D;
  const TKV* Kh = K + (((size_t)b * HKV + kvh) * SK + (size_t)split * TPS * KVBLK) * D;
  const TKV* Vh = V + (((size_t)b * HKV + kvh) * SK + (size_t)split * TPS * KVBLK) * D;
  extern __shared__ __attribute__((aligned(16))) char shm[];
  bf16* V_lds = (bf16*)shm; bf16* K_lds = (bf16*)(shm + 2 * SHM_V);
  float* ws = (float*)(shm + SHM_WS) + wid * 64; float* li_l = ws; float* al_l = ws + 32;
  constexpr int QP = InCfg<TKV>::QP; char* qp = shm + shm_qp<NWT>() + tid * 16;
  const int R = wid * QBLK + r32, Rc = R < ROWS ? R : ROWS - 1;
  int len = SK; if constexpr (HAS_LENS) { len = __builtin_amdgcn_readfirstlane(lens[b]); len = len < 0 ? 0 : (len > SK ? SK : len); }
  const int lim0 = CAUSAL ? len - SQ : len - 1, lim = CAUSAL ? lim0 + Rc % SQ : lim0, kb0 = split * TPS * KVBLK;
  bf16x8 qr[8]; const TQ* qrow = Qg + (size_t)Rc * D;
  float m_reg = -__builtin_inff(), msub = 0.f, l_reg = 0.f; f32x16 o[4] = {};
  const int sr = tid >> 4, sc = (tid & 15) * 8;
  const int vb0 = (int)(uintptr_t)V_lds + (lane & 15) * 8 + ((lane >> 4) & 1) * 1024 + (lane >> 5) * 256;
  Stage<TKV, NR> st; int sk0 = 0; (void)sk0;
#define SLOAD(k0) do { sk0 = kb0 + (k0); \
    if constexpr (NR == 2) st.load(Vh + (size_t)((k0) + sr) * D + sc, Vh + (size_t)((k0) + 32 + sr) * D + sc, Kh + (size_t)((k0) + sr) * D + sc, Kh + (size_t)((k0) + 32 + sr) * D + sc); \
    else { const TKV* vr_ = Vh + (size_t)((k0) + NR * sr) * D + sc; const TKV* kr_ = Kh + (size_t)((k0) + NR * sr) * D + sc; \
           for (int i_ = 0; i_ < NR; ++i_) st.ldv(i_, vr_ + i_ * D); for (int i_ = 0; i_ < NR; ++i_) st.ldk(i_, kr_ + i_ * D); } } while (0)
#define SWRITE(buf) do { if constexpr (NR == 2) { bf16x8 v0_ = st.gv0(), v1_ = st.gv1(), k0_ = st.gk0(), k1_ = st.gk1(); \
      if constexpr (HAS_LENS) { if (sk0 + KVBLK > len) { const bf16x8 z_ = {}; if (sk0 + sr >= len) { v0_ = z_; k0_ = z_; } if (sk0 + 32 + sr >= len) { v1_ = z_; k1_ = z_; } } } \
      *(bf16x8*)((char*)V_lds + (buf) * SHM_V + voff(sr, sc)) = v0_; *(bf16x8*)((char*)V_lds + (buf) * SHM_V + voff(32 + sr, sc)) = v1_; int kc = sc * 2; \
      *(bf16x8*)((char*)K_lds + (buf) * SHM_K + KSWZ(sr, kc)) = k0_; *(bf16x8*)((char*)K_lds + (buf) * SHM_K + KSWZ(32 + sr, kc)) = k1_; } \
    else { for (int i_ = 0; i_ < NR; ++i_) { const int row_ = NR * sr + i_; bf16x8 v_ = st.gv(i_), k_ = st.gk(i_); \
      if constexpr (HAS_LENS) { if (sk0 + KVBLK > len) { if (sk0 + row_ >= len) { v_ = bf16x8{}; k_ = bf16x8{}; } } } \
      *(bf16x8*)((char*)V_lds + (buf) * SHM_V + voff(row_, sc)) = v_; *(bf16x8*)((char*)K_lds + (buf) * SHM_K + KSWZ(row_, sc * 2)) = k_; } } } while (0)
#define RESC(a) do { if (__any((a) < 1.f)) { if constexpr (NWT == NW || !XCHG4) { if (hi == 0) al_l[r32] = (a); asm volatile("s_waitcnt lgkmcnt(0)" ::: "memory"); \
      for (int d = 0; d < 4; ++d) for (int r = 0; r < 16; ++r) o[d][r] *= al_l[crow(r, hi)]; } \
    else { const int ai_ = __float_as_int(a);    \
      for (int r = 0; r < 16; ++r) { const float a0_ = __int_as_float(__builtin_amdgcn_readlane(ai_, crow(r, 0))), a1_ = __int_as_float(__builtin_amdgcn_readlane(ai_, crow(r, 1))); \
        const float ar_ = hi ? a1_ : a0_; for (int d = 0; d < 4; ++d) o[d][r] *= ar_; } } } } while (0)
  f32x16 pA0, pA1, pB0, pB1; float alA, alB; bf16x8 pa0, pa1, pa2, pa3;
  for (int d0 = 0; d0 < 8; ++d0) qr[d0] = ld8(qrow + d0 * 16 + hi * 8);
  for (int j = 0; j < QP; ++j) *reinterpret_cast<bf16x8*>(qp + j * (64 * NWT * 16)) = qr[8 - QP + j];
  SLOAD(0); SWRITE(0); __syncthreads();
  qkt<QP, NWT>(pA0, pA1, K_lds, qr, qp, r32, hi); partialSM(pA0, pA1, m_reg, msub, alA, kb0, lim, lim0);
  SLOAD(KVBLK); SWRITE(1); __syncthreads();
  for (int j = 1; j + 1 < TPS; j += 2) {
    SBAR(); qkt<QP, NWT>(pB0, pB1, (bf16*)((char*)K_lds + SHM_K), qr, qp, r32, hi);
    finishSM(pA0, pA1, alA, l_reg, pa0, pa1, pa2, pa3); SBAR();
    SLOAD((j + 1) * KVBLK); SBAR();
    pv_d0(o, vb0, pa0, pa1, pa2, pa3); partialSM(pB0, pB1, m_reg, msub, alB, kb0 + j * KVBLK, lim, lim0);
    __syncthreads(); asm volatile("s_waitcnt vmcnt(0)" ::: "memory"); SWRITE(0);
    RESC(alB); __syncthreads();
    SBAR(); qkt<QP, NWT>(pA0, pA1, K_lds, qr, qp, r32, hi);
    finishSM(pB0, pB1, alB, l_reg, pa0, pa1, pa2, pa3); SBAR();
    if (j + 2 < TPS) { SLOAD((j + 2) * KVBLK); SBAR(); }
    pv_d0(o, vb0 + (int)SHM_V, pa0, pa1, pa2, pa3); partialSM(pA0, pA1, m_reg, msub, alA, kb0 + (j + 1) * KVBLK, lim, lim0);
    __syncthreads(); if (j + 2 < TPS) { asm volatile("s_waitcnt vmcnt(0)" ::: "memory"); SWRITE(1); }
    RESC(alA); __syncthreads();
  }
  SBAR(); qkt<QP, NWT>(pB0, pB1, (bf16*)((char*)K_lds + SHM_K), qr, qp, r32, hi);
  finishSM(pA0, pA1, alA, l_reg, pa0, pa1, pa2, pa3); SBAR();
  pv_d0(o, vb0, pa0, pa1, pa2, pa3); partialSM(pB0, pB1, m_reg, msub, alB, kb0 + (TPS - 1) * KVBLK, lim, lim0);
  __syncthreads(); RESC(alB);
  finishSM(pB0, pB1, alB, l_reg, pa0, pa1, pa2, pa3); SBAR();
  pv_d0(o, vb0 + (int)SHM_V, pa0, pa1, pa2, pa3);
#undef SLOAD
#undef SWRITE
#undef RESC
  if constexpr (NS == 1) {
    float rli[16];
    if constexpr (NWT == NW || !XCHG4) { if (hi == 0) li_l[r32] = l_reg; asm volatile("s_waitcnt lgkmcnt(0)" ::: "memory");
#pragma unroll
      for (int r = 0; r < 16; ++r) { float l = li_l[crow(r, hi)]; rli[r] = l > 0.f ? __builtin_amdgcn_rcpf(l) : 0.f; } }
    else {
#pragma unroll
      for (int r = 0; r < 16; ++r) { const int li_ = __float_as_int(l_reg); const float l0 = __int_as_float(__builtin_amdgcn_readlane(li_, crow(r, 0))), l1 = __int_as_float(__builtin_amdgcn_readlane(li_, crow(r, 1)));
        const float l = hi ? l1 : l0; rli[r] = l > 0.f ? __builtin_amdgcn_rcpf(l) : 0.f; } }
#pragma unroll
    for (int r = 0; r < 16; ++r) { const int Ro = wid * QBLK + crow(r, hi); if (Ro < ROWS) { float* orow = O + (((size_t)b * HQ + (size_t)kvh * G) * SQ + Ro) * D;
        for (int d0 = 0; d0 < 4; ++d0) orow[d0 * 32 + r32] = o[d0][r] * rli[r]; } }
  } else {
    const size_t row0 = ((size_t)b * HQ + (size_t)kvh * G) * SQ;
    if (hi == 0 && R < ROWS) { float* p = pml + ((row0 + R) * NS + split) * 2; p[0] = m_reg; p[1] = l_reg; }
#pragma unroll
    for (int r = 0; r < 16; ++r) { const int Ro = wid * QBLK + crow(r, hi); if (Ro < ROWS) { float* prow = po + ((row0 + Ro) * NS + split) * D;
        for (int d0 = 0; d0 < 4; ++d0) prow[d0 * 32 + r32] = o[d0][r]; } }
  }
}

constexpr int CMB_ROWS = 8, CMB_BLOCKS = (int)(((size_t)B * HQ * SQ + CMB_ROWS - 1) / CMB_ROWS);
template <int NS> __global__ __launch_bounds__(256) void gqa_chunk_combine(const float* __restrict__ pml, const float* __restrict__ po, bf16* __restrict__ O) {
  const size_t row = (size_t)blockIdx.x * CMB_ROWS + (threadIdx.x >> 5); const int d = (threadIdx.x & 31) * 4;
  if (row >= (size_t)B * HQ * SQ) return;
  const float* ml = pml + row * NS * 2; const float* acc = po + row * NS * D + d;
  float M = -__builtin_inff(); for (int s = 0; s < NS; ++s) M = fmaxf(M, ml[2 * s]);
  const float Msub = (M == -__builtin_inff()) ? 0.f : M; float L = 0.f; f32x4 o = {0.f, 0.f, 0.f, 0.f};
  for (int s = 0; s < NS; ++s) { const float m = ml[2 * s], l = ml[2 * s + 1]; const float w = l > 0.f ? __builtin_amdgcn_exp2f(m - Msub) : 0.f;
    const f32x4 a = *reinterpret_cast<const f32x4*>(acc + (size_t)s * D); L += w * l; o[0] += w * a[0]; o[1] += w * a[1]; o[2] += w * a[2]; o[3] += w * a[3]; }
  const float inv = L > 0.f ? 1.f / L : 0.f; o[0] *= inv; o[1] *= inv; o[2] *= inv; o[3] *= inv;
  uint2 w; w.x = cvtpk(o[0], o[1]); w.y = cvtpk(o[2], o[3]); *reinterpret_cast<uint2*>(O + row * D + d) = w;
}

__global__ __launch_bounds__(256) void cvt_f32_bf16(const float* __restrict__ q, const float* __restrict__ k, const float* __restrict__ v, bf16* __restrict__ qb, bf16* __restrict__ kb, bf16* __restrict__ vb) {
  const size_t i0 = ((size_t)blockIdx.x * 256 + threadIdx.x) * 8, step = (size_t)gridDim.x * 256 * 8;
  for (size_t i = i0; i < NQ; i += step) *reinterpret_cast<bf16x8*>(qb + i) = ld8(q + i);
  for (size_t i = i0; i < NKV; i += step) { *reinterpret_cast<bf16x8*>(kb + i) = ld8(k + i); *reinterpret_cast<bf16x8*>(vb + i) = ld8(v + i); }
}
template <class TQ, class TKV>
static void gqa_chunk_forward(const TQ* q, const TKV* k, const TKV* v, const int* lens, float* o, void* ws, hipStream_t stream) {
  static int lds_set = 0;
  constexpr size_t shm = shm_bytes<TKV>();
  if (lds_set == 0) { lds_set = hipFuncSetAttribute((const void*)gqa_chunk_fwd<TQ, TKV>, hipFuncAttributeMaxDynamicSharedMemorySize, (int)shm) == hipSuccess ? 1 : -1; if (lds_set < 0) fprintf(stderr, "kernel_launch: hipFuncSetAttribute(%zu B LDS) failed\n", shm); }
  if (lds_set < 0) return;
  float* pml = (float*)((char*)ws + WS_PML); float* po = (float*)((char*)ws + WS_PO);
  constexpr int NS = ns_for<TKV>();
  hipLaunchKernelGGL((gqa_chunk_fwd<TQ, TKV>), dim3(NS, HKV, B), dim3(64 * nw_for<TKV>()), shm, stream, q, k, v, lens, o, pml, po);
  if constexpr (NS > 1) hipLaunchKernelGGL((gqa_chunk_combine<NS>), dim3(CMB_BLOCKS), dim3(256), 0, stream, (const float*)pml, (const float*)po, (bf16*)o);
  const hipError_t le = hipPeekAtLastError();
  if (le != hipSuccess) fprintf(stderr, "kernel_launch: launch failed: %s (grid %d x %d x %d, %zu B LDS)\n", hipGetErrorName(le), NS, HKV, B, shm);
}

extern "C" void kernel_launch(void* const* d_in, const int* in_sizes, int n_in, void* d_out, int out_size, void* d_ws, size_t ws_size, hipStream_t stream) {
  static int state = 0;
  if (state == 0) {
    const long nq = (long)B * HQ * SQ * D, nkv = (long)B * HKV * SK * D;
    if (n_in != 3 + HAS_LENS || in_sizes[0] != nq || in_sizes[1] != nkv || in_sizes[2] != nkv || (HAS_LENS && in_sizes[3] != B) || out_size != nq || ws_size < WS_END) {
      fprintf(stderr, "kernel_launch: unexpected problem shape (n_in %d, in %d/%d/%d, out %d, ws %zu; want q %ld, k/v %ld, ws >= %zu)\n", n_in,
              n_in > 0 ? in_sizes[0] : -1, n_in > 1 ? in_sizes[1] : -1, n_in > 2 ? in_sizes[2] : -1, out_size, ws_size, nq, nkv, WS_END);
      state = -1; return; }
    state = 1;
  }
  if (state < 0) return;
  const int* lens = HAS_LENS ? (const int*)d_in[3] : nullptr;
  if constexpr (KV_CONVERT) {
    bf16* qb = (bf16*)((char*)d_ws + WS_QB); bf16* kb = (bf16*)((char*)d_ws + WS_KB); bf16* vb = (bf16*)((char*)d_ws + WS_VB);
    hipLaunchKernelGGL(cvt_f32_bf16, dim3(2048), dim3(256), 0, stream, (const float*)d_in[0], (const float*)d_in[1], (const float*)d_in[2], qb, kb, vb);
    gqa_chunk_forward<bf16, bf16>((const bf16*)qb, (const bf16*)kb, (const bf16*)vb, lens, (float*)d_out, d_ws, stream);
  } else {
    gqa_chunk_forward<bf16, bf16>((const bf16*)d_in[0], (const bf16*)d_in[1], (const bf16*)d_in[2], lens, (float*)d_out, d_ws, stream);
  }
}
```

```cpp
#include <hip/hip_runtime.h>
#include <hip/hip_bf16.h>
#include <cstdio>

#define ATTN_B 8
#define ATTN_HQ 32
#define ATTN_HKV 4
#define ATTN_SQ 32
#define ATTN_SK 2048
#define ATTN_NSPLIT 8
#define ATTN_THR 8
#define ATTN_KVCONV 0
#define ATTN_CAUSAL 0
#define ATTN_LENS 0
#define ATTN_QPARK (4 + ATTN_LENS)
#define ATTN_WG4 1
#define ATTN_QPARK4 4
#define ATTN_XCHG4 1
constexpr int B = ATTN_B, HQ = ATTN_HQ, HKV = ATTN_HKV, SQ = ATTN_SQ, SK = ATTN_SK;
constexpr int NSPLIT = ATTN_NSPLIT;
constexpr float THR = ATTN_THR;
constexpr int CAUSAL = ATTN_CAUSAL;
constexpr int HAS_LENS = ATTN_LENS;
constexpr int KV_CONVERT = ATTN_KVCONV;
constexpr int QPARK = ATTN_QPARK;
constexpr int WG4 = ATTN_WG4;
constexpr int XCHG4 = ATTN_XCHG4;
constexpr int QPARK4 = ATTN_QPARK4;
constexpr int D = 128, NW = 8, QBLK = 32, KVBLK = 64;
constexpr float SCALE = 0.088388347648318447f;
constexpr int G = HQ / HKV, ROWS = G * SQ, NT = SK / KVBLK, TPS = NT / NSPLIT;
constexpr int NWB = (WG4 && ROWS <= QBLK * 4 && !CAUSAL && !HAS_LENS && TPS % 4 == 0) ? 4 : NW, NSB = NSPLIT * (NW / NWB), TPSB = NT / NSB;
static_assert(HQ % HKV == 0 && ROWS >= 1 && ROWS <= QBLK * NW, "G*SQ must fit the workgroup's 256 MFMA rows (larger SQ -> dense body)");
static_assert(!(CAUSAL && HAS_LENS && !KV_CONVERT), "CAUSAL + HAS_LENS on the f32 convert-on-load loop spills 2 VGPRs (ROCm 7.2, QPARK 4..8): set KV_CONVERT 1 (bf16 loop, 0 spill) for that combination");
static_assert(SK % KVBLK == 0 && NT % NSPLIT == 0 && TPS >= 2 && TPS % 2 == 0, "SK must give every split an even number (>= 2) of 64-key tiles");
static_assert(NT % NSB == 0 && TPSB >= 2 && TPSB % 2 == 0, "internal: half-split tile count (NWB falls back to 8 waves when TPS % 4 != 0)");
constexpr float L2E = 1.4426950408889634f, SL2E = SCALE * L2E, THR2 = THR * L2E;
constexpr size_t SHM_V = KVBLK * D * 2, SHM_K = KVBLK * D * 2, SHM_WS = 2 * SHM_V + 2 * SHM_K;
template <int NWT> constexpr size_t shm_qp() { return SHM_WS + ((NWT == NW || !XCHG4) ? (size_t)NWT * 64 * 4 : 0); }
static_assert(QPARK >= 0 && QPARK <= 8 && QPARK4 >= 0 && QPARK4 <= 8, "QPARK");
constexpr size_t WS_PML = 4096, PML_BYTES = (size_t)B * HQ * SQ * NSB * 2 * 4;
constexpr size_t WS_PO = (WS_PML + PML_BYTES + 255) / 256 * 256, WS_PEND = NSB > 1 ? WS_PO + (size_t)B * HQ * SQ * NSB * D * 4 : WS_PML;
constexpr size_t NQ = (size_t)B * HQ * SQ * D, NKV = (size_t)B * HKV * SK * D;
constexpr size_t WS_QB = (WS_PEND + 255) / 256 * 256, WS_KB = WS_QB + NQ * 2, WS_VB = WS_KB + NKV * 2;
constexpr size_t WS_END = KV_CONVERT ? WS_VB + NKV * 2 : WS_PEND;

using bf16 = __hip_bfloat16;
using bf16x8 = __attribute__((ext_vector_type(8))) short;
using s16x4 = __attribute__((ext_vector_type(4))) short;
using f32x16 = __attribute__((ext_vector_type(16))) float;
using f32x4 = __attribute__((ext_vector_type(4))) float;
using u32x4 = __attribute__((ext_vector_type(4))) unsigned;
template <class T> struct InCfg;
template <> struct InCfg<__hip_bfloat16> { static constexpr int NWT = NWB, QP = NWB == 4 ? QPARK4 : 0; };
template <> struct InCfg<float> { static constexpr int NWT = NW, QP = QPARK; };
template <class T> constexpr int nw_for() { return InCfg<T>::NWT; }
template <class T> constexpr int ns_for() { return NSPLIT * (NW / InCfg<T>::NWT); }
template <class T> constexpr size_t shm_bytes() { return shm_qp<InCfg<T>::NWT>() + (size_t)InCfg<T>::QP * 64 * InCfg<T>::NWT * 16; }
#define KSWZ(row, colB) ((row) * 256 + ((colB) ^ (((row) & 7) << 4)))
#define SBAR() __builtin_amdgcn_sched_barrier(0)
__device__ __forceinline__ int crow(int r, int hi) { return (r & 3) + 8 * (r >> 2) + 4 * hi; }
__device__ __forceinline__ unsigned cvtpk(float lo, float hi) { unsigned r; asm volatile("v_cvt_pk_bf16_f32 %0, %1, %2" : "=v"(r) : "v"(lo), "v"(hi)); return r; }
__device__ __forceinline__ bf16x8 pack8(f32x4 a, f32x4 b) { u32x4 w = {cvtpk(a[0], a[1]), cvtpk(a[2], a[3]), cvtpk(b[0], b[1]), cvtpk(b[2], b[3])}; return *reinterpret_cast<bf16x8*>(&w); }
__device__ __forceinline__ bf16x8 ld8(const bf16* p) { return *reinterpret_cast<const bf16x8*>(p); }
__device__ __forceinline__ bf16x8 ld8(const float* p) { return pack8(*reinterpret_cast<const f32x4*>(p), *reinterpret_cast<const f32x4*>(p + 4)); }
template <class T, int NR> struct Stage;
template <> struct Stage<bf16, 2> { bf16x8 v0, v1, k0, k1;
  __device__ __forceinline__ void load(const bf16* V0, const bf16* V1, const bf16* K0, const bf16* K1) { v0 = ld8(V0); v1 = ld8(V1); k0 = ld8(K0); k1 = ld8(K1); }
  __device__ __forceinline__ bf16x8 gv0() const { return v0; } __device__ __forceinline__ bf16x8 gv1() const { return v1; }
  __device__ __forceinline__ bf16x8 gk0() const { return k0; } __device__ __forceinline__ bf16x8 gk1() const { return k1; } };
template <> struct Stage<float, 2> { f32x4 v0a, v0b, v1a, v1b, k0a, k0b, k1a, k1b;
  __device__ __forceinline__ void load(const float* V0, const float* V1, const float* K0, const float* K1) {
    v0a = *(const f32x4*)V0; v0b = *(const f32x4*)(V0 + 4); v1a = *(const f32x4*)V1; v1b = *(const f32x4*)(V1 + 4);
    k0a = *(const f32x4*)K0; k0b = *(const f32x4*)(K0 + 4); k1a = *(const f32x4*)K1; k1b = *(const f32x4*)(K1 + 4); }
  __device__ __forceinline__ bf16x8 gv0() const { return pack8(v0a, v0b); } __device__ __forceinline__ bf16x8 gv1() const { return pack8(v1a, v1b); }
  __device__ __forceinline__ bf16x8 gk0() const { return pack8(k0a, k0b); } __device__ __forceinline__ bf16x8 gk1() const { return pack8(k1a, k1b); } };
template <int NR> struct Stage<bf16, NR> { bf16x8 v[NR], k[NR];
  __device__ __forceinline__ void ldv(int i, const bf16* p) { v[i] = ld8(p); } __device__ __forceinline__ void ldk(int i, const bf16* p) { k[i] = ld8(p); }
  __device__ __forceinline__ bf16x8 gv(int i) const { return v[i]; } __device__ __forceinline__ bf16x8 gk(int i) const { return k[i]; } };
template <int NR> struct Stage<float, NR> { f32x4 va[NR], vb[NR], ka[NR], kb[NR];
  __device__ __forceinline__ void ldv(int i, const float* p) { va[i] = *(const f32x4*)p; vb[i] = *(const f32x4*)(p + 4); }
  __device__ __forceinline__ void ldk(int i, const float* p) { ka[i] = *(const f32x4*)p; kb[i] = *(const f32x4*)(p + 4); }
  __device__ __forceinline__ bf16x8 gv(int i) const { return pack8(va[i], vb[i]); } __device__ __forceinline__ bf16x8 gk(int i) const { return pack8(ka[i], kb[i]); } };
__device__ __forceinline__ int voff(int row, int sc) { return (row >> 5) * 8192 + 2 * ((sc >> 4) * 512 + (row & 31) * 16 + (sc & 8)); }

__device__ __forceinline__ void partialSM(f32x16& p0, f32x16& p1, float& m_reg, float& msub, float& alpha, int kbase, int lim, int lim0) {
  if constexpr (CAUSAL || HAS_LENS) { if (kbase + KVBLK - 1 > lim0) {
    const float ninf = -__builtin_inff(); const int hi = (threadIdx.x & 63) >> 5;
    for (int r = 0; r < 16; ++r) { const int k = kbase + crow(r, hi); p0[r] = k > lim ? ninf : p0[r]; p1[r] = k + 32 > lim ? ninf : p1[r]; } } }
  float pmax = p0[0]; for (int r = 1; r < 16; ++r) pmax = fmaxf(pmax, p0[r]); for (int r = 0; r < 16; ++r) pmax = fmaxf(pmax, p1[r]);
  { auto rr = __builtin_amdgcn_permlane32_swap(__float_as_uint(pmax), __float_as_uint(pmax), false, false);
    pmax = fmaxf(__uint_as_float(rr[0]), __uint_as_float(rr[1])); }
  pmax *= SL2E;
  if (__builtin_expect(__all(pmax - m_reg <= THR2), 1)) { alpha = 1.f; }
  else { float mn = fmaxf(m_reg, pmax); msub = (mn == -__builtin_inff()) ? 0.f : mn; alpha = __builtin_amdgcn_exp2f(m_reg - msub); m_reg = mn; }
  float nm = -msub;
  for (int r = 0; r < 16; ++r) p0[r] = fmaf(p0[r], SL2E, nm); for (int r = 0; r < 16; ++r) p1[r] = fmaf(p1[r], SL2E, nm);
  for (int r = 0; r < 16; ++r) p0[r] = __builtin_amdgcn_exp2f(p0[r]);
}
__device__ __forceinline__ void finishSM(f32x16& p0, f32x16& p1, float alpha, float& l_reg, bf16x8& pa0, bf16x8& pa1, bf16x8& pa2, bf16x8& pa3) {
  for (int r = 0; r < 16; ++r) p1[r] = __builtin_amdgcn_exp2f(p1[r]);
  float ps = 0; for (int r = 0; r < 16; ++r) ps += p0[r]; for (int r = 0; r < 16; ++r) ps += p1[r];
  { auto rr = __builtin_amdgcn_permlane32_swap(__float_as_uint(ps), __float_as_uint(ps), false, false); ps = __uint_as_float(rr[0]) + __uint_as_float(rr[1]); }
  l_reg = l_reg * alpha + ps;
#define PK4(P, BASE, OUT) do { unsigned a0 = cvtpk(P[BASE + 0], P[BASE + 1]), a1 = cvtpk(P[BASE + 2], P[BASE + 3]); \
    unsigned b0 = cvtpk(P[BASE + 4], P[BASE + 5]), b1 = cvtpk(P[BASE + 6], P[BASE + 7]); \
    auto r0 = __builtin_amdgcn_permlane32_swap(a0, b0, false, false); auto r1 = __builtin_amdgcn_permlane32_swap(a1, b1, false, false); \
    u32x4 w = {r0[0], r1[0], r0[1], r1[1]}; OUT = *reinterpret_cast<bf16x8*>(&w); } while (0)
  PK4(p0, 0, pa0); PK4(p0, 8, pa1); PK4(p1, 0, pa2); PK4(p1, 8, pa3);
#undef PK4
}
template <int QP, int NWT> __device__ __forceinline__ void qkt(f32x16& p0, f32x16& p1, const bf16* Ks, const bf16x8* qr, const char* qp, int r32, int hi) {
  p0 = f32x16{}; p1 = f32x16{};
#pragma unroll
  for (int d0 = 0; d0 < 8; ++d0) { int cb = (d0 * 16 + hi * 8) * 2;
    bf16x8 b0 = *reinterpret_cast<const bf16x8*>((const char*)Ks + KSWZ(r32, cb));
    bf16x8 b1 = *reinterpret_cast<const bf16x8*>((const char*)Ks + KSWZ(32 + r32, cb));
    const bf16x8 q = d0 < 8 - QP ? qr[d0] : *reinterpret_cast<const bf16x8*>(qp + (d0 - (8 - QP)) * (64 * NWT * 16));
    p0 = __builtin_amdgcn_mfma_f32_32x32x16_bf16(b0, q, p0, 0, 0, 0);
    p1 = __builtin_amdgcn_mfma_f32_32x32x16_bf16(b1, q, p1, 0, 0, 0); }
}
__device__ __forceinline__ void pv_d0(f32x16* o, int vb, bf16x8 pa0, bf16x8 pa1, bf16x8 pa2, bf16x8 pa3) {
#pragma unroll
  for (int d0 = 0; d0 < 4; ++d0) { s16x4 lo[4], hi[4];
#pragma unroll
    for (int ks = 0; ks < 4; ++ks) { int a = vb + d0 * 2048 + (ks >> 1) * 8192 + (ks & 1) * 512;
      asm volatile("ds_read_b64_tr_b16 %0,%1" : "=&v"(lo[ks]) : "v"(a) : "memory");
      asm volatile("ds_read_b64_tr_b16 %0,%1 offset:128" : "=&v"(hi[ks]) : "v"(a) : "memory"); }
    asm volatile("s_waitcnt lgkmcnt(0)" ::: "memory"); SBAR();
#define PK(k) (bf16x8){lo[k][0], lo[k][1], lo[k][2], lo[k][3], hi[k][0], hi[k][1], hi[k][2], hi[k][3]}
    o[d0] = __builtin_amdgcn_mfma_f32_32x32x16_bf16(pa0, PK(0), o[d0], 0, 0, 0);
    o[d0] = __builtin_amdgcn_mfma_f32_32x32x16_bf16(pa1, PK(1), o[d0], 0, 0, 0);
    o[d0] = __builtin_amdgcn_mfma_f32_32x32x16_bf16(pa2, PK(2), o[d0], 0, 0, 0);
    o[d0] = __builtin_amdgcn_mfma_f32_32x32x16_bf16(pa3, PK(3), o[d0], 0, 0, 0);
#undef PK
  }
}

template <class TQ, class TKV>
__global__ __launch_bounds__(64 * InCfg<TKV>::NWT, NW / InCfg<TKV>::NWT)
void gqa_chunk_fwd(const TQ* __restrict__ Q, const TKV* __restrict__ K, const TKV* __restrict__ V, const int* __restrict__ lens, float* __restrict__ O, float* __restrict__ pml, float* __restrict__ po) {
  constexpr int NWT = InCfg<TKV>::NWT, NS = NSPLIT * (NW / NWT), TPS = NT / NS;
  constexpr int NR = KVBLK / (4 * NWT);
  static_assert(ROWS <= QBLK * NWT, "rows must fit this form's waves");
  const int tid = threadIdx.x, wid = tid >> 6, lane = tid & 63, r32 = lane & 31, hi = lane >> 5;
  const int split = blockIdx.x, kvh = blockIdx.y, b = blockIdx.z;
  const TQ* Qg = Q + ((size_t)b * HQ + (size_t)kvh * G) * SQ * D;
  const TKV* Kh = K + (((size_t)b * HKV + kvh) * SK + (size_t)split * TPS * KVBLK) * D;
  const TKV* Vh = V + (((size_t)b * HKV + kvh) * SK + (size_t)split * TPS * KVBLK) * D;
  extern __shared__ __attribute__((aligned(16))) char shm[];
  bf16* V_lds = (bf16*)shm; bf16* K_lds = (bf16*)(shm + 2 * SHM_V);
  float* ws = (float*)(shm + SHM_WS) + wid * 64; float* li_l = ws; float* al_l = ws + 32;
  constexpr int QP = InCfg<TKV>::QP; char* qp = shm + shm_qp<NWT>() + tid * 16;
  const int R = wid * QBLK + r32, Rc = R < ROWS ? R : ROWS - 1;
  int len = SK; if constexpr (HAS_LENS) { len = __builtin_amdgcn_readfirstlane(lens[b]); len = len < 0 ? 0 : (len > SK ? SK : len); }
  const int lim0 = CAUSAL ? len - SQ : len - 1, lim = CAUSAL ? lim0 + Rc % SQ : lim0, kb0 = split * TPS * KVBLK;
  bf16x8 qr[8]; const TQ* qrow = Qg + (size_t)Rc * D;
  float m_reg = -__builtin_inff(), msub = 0.f, l_reg = 0.f; f32x16 o[4] = {};
  const int sr = tid >> 4, sc = (tid & 15) * 8;
  const int vb0 = (int)(uintptr_t)V_lds + (lane & 15) * 8 + ((lane >> 4) & 1) * 1024 + (lane >> 5) * 256;
  Stage<TKV, NR> st; int sk0 = 0; (void)sk0;
#define SLOAD(k0) do { sk0 = kb0 + (k0); \
    if constexpr (NR == 2) st.load(Vh + (size_t)((k0) + sr) * D + sc, Vh + (size_t)((k0) + 32 + sr) * D + sc, Kh + (size_t)((k0) + sr) * D + sc, Kh + (size_t)((k0) + 32 + sr) * D + sc); \
    else { const TKV* vr_ = Vh + (size_t)((k0) + NR * sr) * D + sc; const TKV* kr_ = Kh + (size_t)((k0) + NR * sr) * D + sc; \
           for (int i_ = 0; i_ < NR; ++i_) st.ldv(i_, vr_ + i_ * D); for (int i_ = 0; i_ < NR; ++i_) st.ldk(i_, kr_ + i_ * D); } } while (0)
#define SWRITE(buf) do { if constexpr (NR == 2) { bf16x8 v0_ = st.gv0(), v1_ = st.gv1(), k0_ = st.gk0(), k1_ = st.gk1(); \
      if constexpr (HAS_LENS) { if (sk0 + KVBLK > len) { const bf16x8 z_ = {}; if (sk0 + sr >= len) { v0_ = z_; k0_ = z_; } if (sk0 + 32 + sr >= len) { v1_ = z_; k1_ = z_; } } } \
      *(bf16x8*)((char*)V_lds + (buf) * SHM_V + voff(sr, sc)) = v0_; *(bf16x8*)((char*)V_lds + (buf) * SHM_V + voff(32 + sr, sc)) = v1_; int kc = sc * 2; \
      *(bf16x8*)((char*)K_lds + (buf) * SHM_K + KSWZ(sr, kc)) = k0_; *(bf16x8*)((char*)K_lds + (buf) * SHM_K + KSWZ(32 + sr, kc)) = k1_; } \
    else { for (int i_ = 0; i_ < NR; ++i_) { const int row_ = NR * sr + i_; bf16x8 v_ = st.gv(i_), k_ = st.gk(i_); \
      if constexpr (HAS_LENS) { if (sk0 + KVBLK > len) { if (sk0 + row_ >= len) { v_ = bf16x8{}; k_ = bf16x8{}; } } } \
      *(bf16x8*)((char*)V_lds + (buf) * SHM_V + voff(row_, sc)) = v_; *(bf16x8*)((char*)K_lds + (buf) * SHM_K + KSWZ(row_, sc * 2)) = k_; } } } while (0)
#define RESC(a) do { if (__any((a) < 1.f)) { if constexpr (NWT == NW || !XCHG4) { if (hi == 0) al_l[r32] = (a); asm volatile("s_waitcnt lgkmcnt(0)" ::: "memory"); \
      for (int d = 0; d < 4; ++d) for (int r = 0; r < 16; ++r) o[d][r] *= al_l[crow(r, hi)]; } \
    else { const int ai_ = __float_as_int(a);    \
      for (int r = 0; r < 16; ++r) { const float a0_ = __int_as_float(__builtin_amdgcn_readlane(ai_, crow(r, 0))), a1_ = __int_as_float(__builtin_amdgcn_readlane(ai_, crow(r, 1))); \
        const float ar_ = hi ? a1_ : a0_; for (int d = 0; d < 4; ++d) o[d][r] *= ar_; } } } } while (0)
  f32x16 pA0, pA1, pB0, pB1; float alA, alB; bf16x8 pa0, pa1, pa2, pa3;
  for (int d0 = 0; d0 < 8; ++d0) qr[d0] = ld8(qrow + d0 * 16 + hi * 8);
  for (int j = 0; j < QP; ++j) *reinterpret_cast<bf16x8*>(qp + j * (64 * NWT * 16)) = qr[8 - QP + j];
  SLOAD(0); SWRITE(0); __syncthreads();
  qkt<QP, NWT>(pA0, pA1, K_lds, qr, qp, r32, hi); partialSM(pA0, pA1, m_reg, msub, alA, kb0, lim, lim0);
  SLOAD(KVBLK); SWRITE(1); __syncthreads();
  for (int j = 1; j + 1 < TPS; j += 2) {
    SBAR(); qkt<QP, NWT>(pB0, pB1, (bf16*)((char*)K_lds + SHM_K), qr, qp, r32, hi);
    finishSM(pA0, pA1, alA, l_reg, pa0, pa1, pa2, pa3); SBAR();
    SLOAD((j + 1) * KVBLK); SBAR();
    pv_d0(o, vb0, pa0, pa1, pa2, pa3); partialSM(pB0, pB1, m_reg, msub, alB, kb0 + j * KVBLK, lim, lim0);
    __syncthreads(); asm volatile("s_waitcnt vmcnt(0)" ::: "memory"); SWRITE(0);
    RESC(alB); __syncthreads();
    SBAR(); qkt<QP, NWT>(pA0, pA1, K_lds, qr, qp, r32, hi);
    finishSM(pB0, pB1, alB, l_reg, pa0, pa1, pa2, pa3); SBAR();
    if (j + 2 < TPS) { SLOAD((j + 2) * KVBLK); SBAR(); }
    pv_d0(o, vb0 + (int)SHM_V, pa0, pa1, pa2, pa3); partialSM(pA0, pA1, m_reg, msub, alA, kb0 + (j + 1) * KVBLK, lim, lim0);
    __syncthreads(); if (j + 2 < TPS) { asm volatile("s_waitcnt vmcnt(0)" ::: "memory"); SWRITE(1); }
    RESC(alA); __syncthreads();
  }
  SBAR(); qkt<QP, NWT>(pB0, pB1, (bf16*)((char*)K_lds + SHM_K), qr, qp, r32, hi);
  finishSM(pA0, pA1, alA, l_reg, pa0, pa1, pa2, pa3); SBAR();
  pv_d0(o, vb0, pa0, pa1, pa2, pa3); partialSM(pB0, pB1, m_reg, msub, alB, kb0 + (TPS - 1) * KVBLK, lim, lim0);
  __syncthreads(); RESC(alB);
  finishSM(pB0, pB1, alB, l_reg, pa0, pa1, pa2, pa3); SBAR();
  pv_d0(o, vb0 + (int)SHM_V, pa0, pa1, pa2, pa3);
#undef SLOAD
#undef SWRITE
#undef RESC
  if constexpr (NS == 1) {
    float rli[16];
    if constexpr (NWT == NW || !XCHG4) { if (hi == 0) li_l[r32] = l_reg; asm volatile("s_waitcnt lgkmcnt(0)" ::: "memory");
#pragma unroll
      for (int r = 0; r < 16; ++r) { float l = li_l[crow(r, hi)]; rli[r] = l > 0.f ? __builtin_amdgcn_rcpf(l) : 0.f; } }
    else {
#pragma unroll
      for (int r = 0; r < 16; ++r) { const int li_ = __float_as_int(l_reg); const float l0 = __int_as_float(__builtin_amdgcn_readlane(li_, crow(r, 0))), l1 = __int_as_float(__builtin_amdgcn_readlane(li_, crow(r, 1)));
        const float l = hi ? l1 : l0; rli[r] = l > 0.f ? __builtin_amdgcn_rcpf(l) : 0.f; } }
#pragma unroll
    for (int r = 0; r < 16; ++r) { const int Ro = wid * QBLK + crow(r, hi); if (Ro < ROWS) { float* orow = O + (((size_t)b * HQ + (size_t)kvh * G) * SQ + Ro) * D;
        for (int d0 = 0; d0 < 4; ++d0) orow[d0 * 32 + r32] = o[d0][r] * rli[r]; } }
  } else {
    const size_t row0 = ((size_t)b * HQ + (size_t)kvh * G) * SQ;
    if (hi == 0 && R < ROWS) { float* p = pml + ((row0 + R) * NS + split) * 2; p[0] = m_reg; p[1] = l_reg; }
#pragma unroll
    for (int r = 0; r < 16; ++r) { const int Ro = wid * QBLK + crow(r, hi); if (Ro < ROWS) { float* prow = po + ((row0 + Ro) * NS + split) * D;
        for (int d0 = 0; d0 < 4; ++d0) prow[d0 * 32 + r32] = o[d0][r]; } }
  }
}

constexpr int CMB_ROWS = 8, CMB_BLOCKS = (int)(((size_t)B * HQ * SQ + CMB_ROWS - 1) / CMB_ROWS);
template <int NS> __global__ __launch_bounds__(256) void gqa_chunk_combine(const float* __restrict__ pml, const float* __restrict__ po, bf16* __restrict__ O) {
  const size_t row = (size_t)blockIdx.x * CMB_ROWS + (threadIdx.x >> 5); const int d = (threadIdx.x & 31) * 4;
  if (row >= (size_t)B * HQ * SQ) return;
  const float* ml = pml + row * NS * 2; const float* acc = po + row * NS * D + d;
  float M = -__builtin_inff(); for (int s = 0; s < NS; ++s) M = fmaxf(M, ml[2 * s]);
  const float Msub = (M == -__builtin_inff()) ? 0.f : M; float L = 0.f; f32x4 o = {0.f, 0.f, 0.f, 0.f};
  for (int s = 0; s < NS; ++s) { const float m = ml[2 * s], l = ml[2 * s + 1]; const float w = l > 0.f ? __builtin_amdgcn_exp2f(m - Msub) : 0.f;
    const f32x4 a = *reinterpret_cast<const f32x4*>(acc + (size_t)s * D); L += w * l; o[0] += w * a[0]; o[1] += w * a[1]; o[2] += w * a[2]; o[3] += w * a[3]; }
  const float inv = L > 0.f ? 1.f / L : 0.f; o[0] *= inv; o[1] *= inv; o[2] *= inv; o[3] *= inv;
  uint2 w; w.x = cvtpk(o[0], o[1]); w.y = cvtpk(o[2], o[3]); *reinterpret_cast<uint2*>(O + row * D + d) = w;
}

__global__ __launch_bounds__(256) void cvt_f32_bf16(const float* __restrict__ q, const float* __restrict__ k, const float* __restrict__ v, bf16* __restrict__ qb, bf16* __restrict__ kb, bf16* __restrict__ vb) {
  const size_t i0 = ((size_t)blockIdx.x * 256 + threadIdx.x) * 8, step = (size_t)gridDim.x * 256 * 8;
  for (size_t i = i0; i < NQ; i += step) *reinterpret_cast<bf16x8*>(qb + i) = ld8(q + i);
  for (size_t i = i0; i < NKV; i += step) { *reinterpret_cast<bf16x8*>(kb + i) = ld8(k + i); *reinterpret_cast<bf16x8*>(vb + i) = ld8(v + i); }
}
template <class TQ, class TKV>
static void gqa_chunk_forward(const TQ* q, const TKV* k, const TKV* v, const int* lens, float* o, void* ws, hipStream_t stream) {
  static int lds_set = 0;
  constexpr size_t shm = shm_bytes<TKV>();
  if (lds_set == 0) { lds_set = hipFuncSetAttribute((const void*)gqa_chunk_fwd<TQ, TKV>, hipFuncAttributeMaxDynamicSharedMemorySize, (int)shm) == hipSuccess ? 1 : -1; if (lds_set < 0) fprintf(stderr, "kernel_launch: hipFuncSetAttribute(%zu B LDS) failed\n", shm); }
  if (lds_set < 0) return;
  float* pml = (float*)((char*)ws + WS_PML); float* po = (float*)((char*)ws + WS_PO);
  constexpr int NS = ns_for<TKV>();
  hipLaunchKernelGGL((gqa_chunk_fwd<TQ, TKV>), dim3(NS, HKV, B), dim3(64 * nw_for<TKV>()), shm, stream, q, k, v, lens, o, pml, po);
  if constexpr (NS > 1) hipLaunchKernelGGL((gqa_chunk_combine<NS>), dim3(CMB_BLOCKS), dim3(256), 0, stream, (const float*)pml, (const float*)po, (bf16*)o);
  const hipError_t le = hipPeekAtLastError();
  if (le != hipSuccess) fprintf(stderr, "kernel_launch: launch failed: %s (grid %d x %d x %d, %zu B LDS)\n", hipGetErrorName(le), NS, HKV, B, shm);
}

typedef unsigned u32x4_ __attribute__((ext_vector_type(4)));
template<int NT, int A>
__global__ __launch_bounds__(512) void probe(const char* __restrict__ K, const char* __restrict__ V, unsigned* sink) {
  extern __shared__ __attribute__((aligned(16))) char lds[];
  constexpr int NW = 8;
  unsigned long long t0 = __builtin_amdgcn_s_memrealtime();
  const int bid = blockIdx.x, x = bid & 7, i = bid >> 3;
  const int pair = x * 4 + (i >> 3), j = i & 7, split = j / A;
  constexpr int BYTES = 65536 * A;
  const char* kp = K + (size_t)pair * 524288 + (size_t)split * BYTES;
  const char* vp = V + (size_t)pair * 524288 + (size_t)split * BYTES;
  const int wid = __builtin_amdgcn_readfirstlane(threadIdx.x >> 6), lane = threadIdx.x & 63;
  constexpr int NL = BYTES / 1024 / NW;
  constexpr int RING = 16, DEPTH = 16;
  unsigned ldsbase = (unsigned)(size_t)lds + wid * RING * 1024;
  for (int n = 0; n < 2 * NL; ++n) {
    const char* base = (n & 1) ? vp : kp; int m = n >> 1;
    size_t off = (size_t)(m * NW + wid) * 1024;
    const char* g = base + off + lane * 16;
    unsigned dst = __builtin_amdgcn_readfirstlane(ldsbase + (n % RING) * 1024);
    if constexpr (NT == 0) asm volatile("s_mov_b32 m0, %1\n\ts_nop 0\n\tglobal_load_lds_dwordx4 %0, off" :: "v"(g), "s"(dst) : "memory", "m0");
    if constexpr (NT == 1) asm volatile("s_mov_b32 m0, %1\n\ts_nop 0\n\tglobal_load_lds_dwordx4 %0, off nt" :: "v"(g), "s"(dst) : "memory", "m0");
    if (n >= DEPTH - 1) asm volatile("s_waitcnt vmcnt(%0)" :: "i"(DEPTH - 1) : "memory");
  }
  asm volatile("s_waitcnt vmcnt(0)" ::: "memory");
  __syncthreads();
  unsigned long long t1 = __builtin_amdgcn_s_memrealtime();
  unsigned long long tend = t0 + (t1 - t0) * 11;
  while (__builtin_amdgcn_s_memrealtime() < tend) __builtin_amdgcn_s_sleep(8);
  u32x4_ acc = *(u32x4_*)(lds + threadIdx.x * 16);
  if ((acc[0] ^ acc[1] ^ acc[2] ^ acc[3]) == 0x12345678u) sink[threadIdx.x] = 1;
}
extern "C" void kernel_launch(void* const* d_in, const int* in_sizes, int n_in, void* d_out, int out_size, void* d_ws, size_t ws_size, hipStream_t stream) {
  static int state = 0;
  if (state == 0) {
    const long nq = (long)B * HQ * SQ * D, nkv = (long)B * HKV * SK * D;
    if (n_in != 3 + HAS_LENS || in_sizes[0] != nq || in_sizes[1] != nkv || in_sizes[2] != nkv || (HAS_LENS && in_sizes[3] != B) || out_size != nq || ws_size < WS_END) {
      fprintf(stderr, "kernel_launch: unexpected problem shape (n_in %d, in %d/%d/%d, out %d, ws %zu; want q %ld, k/v %ld, ws >= %zu)\n", n_in,
              n_in > 0 ? in_sizes[0] : -1, n_in > 1 ? in_sizes[1] : -1, n_in > 2 ? in_sizes[2] : -1, out_size, ws_size, nq, nkv, WS_END);
      state = -1; return; }
    state = 1;
  }
  if (state < 0) return;
  const int* lens = HAS_LENS ? (const int*)d_in[3] : nullptr;
  hipFuncSetAttribute((const void*)probe<1,1>, hipFuncAttributeMaxDynamicSharedMemorySize, 131072);
  probe<1,1><<<256, 512, 131072, stream>>>((const char*)d_in[1], (const char*)d_in[2], (unsigned*)d_ws + (200u<<18));
  if constexpr (KV_CONVERT) {
    bf16* qb = (bf16*)((char*)d_ws + WS_QB); bf16* kb = (bf16*)((char*)d_ws + WS_KB); bf16* vb = (bf16*)((char*)d_ws + WS_VB);
    hipLaunchKernelGGL(cvt_f32_bf16, dim3(2048), dim3(256), 0, stream, (const float*)d_in[0], (const float*)d_in[1], (const float*)d_in[2], qb, kb, vb);
    gqa_chunk_forward<bf16, bf16>((const bf16*)qb, (const bf16*)kb, (const bf16*)vb, lens, (float*)d_out, d_ws, stream);
  } else {
    gqa_chunk_forward<bf16, bf16>((const bf16*)d_in[0], (const bf16*)d_in[1], (const bf16*)d_in[2], lens, (float*)d_out, d_ws, stream);
  }
}
```

```cpp
#include <hip/hip_runtime.h>
#include <stdint.h>
constexpr unsigned LDS_BYTES = 131072 + 512;
__global__ __launch_bounds__(384) void gqa_fwd(const char* Q, const char* K, const char* V, char* O, char* ws ) {
  __shared__ __attribute__((aligned(16))) char lds[LDS_BYTES];
  asm volatile(
    "s_load_dwordx8 s[8:15], %[karg], 0\n\t"
    "s_load_dwordx2 s[16:17], %[karg], 32\n\t"
    "s_getpc_b64 s[38:39]\n\t"
    ".Lbase%=:\n\t"
    "v_and_b32 v1, 63, v0\n\t"
    "v_lshrrev_b32 v2, 6, v0\n\t"
    "s_lshr_b32 s26, %[bid], 3\n\t"
    "v_readfirstlane_b32 s20, v2\n\t"
    "s_and_b32 s21, %[bid], 7\n\t"
    "s_lshl_b32 s21, s21, 2\n\t"
    "s_lshr_b32 s27, s26, 3\n\t"
    "s_add_u32 s21, s21, s27\n\t"
    "s_bfe_u32 s22, s26, 0x20001\n\t"
    "s_and_b32 s23, s26, 1\n\t"
    "s_xor_b32 s24, s22, 3\n\t"
    "s_lshl_b32 s25, s21, 19\n\t"
    "s_lshl_b32 s27, s22, 17\n\t"
    "s_add_u32 s25, s25, s27\n\t"
    "s_cmp_eq_u32 s20, 5\n\t"
    "s_cbranch_scc1 .Lhelp%=\n\t"
    "v_bfe_u32 v10, v1, 2, 3\n\t"
    "v_lshlrev_b32 v10, 8, v10\n\t"
    "v_lshrrev_b32 v11, 5, v1\n\t"
    "v_bfe_u32 v12, v1, 4, 1\n\t"
    "v_and_b32 v13, 3, v1\n\t"
    "v_mul_u32_u24 v14, 3, v12\n\t"
    "v_xor_b32 v14, v13, v14\n\t"
    "v_lshl_add_u32 v15, v11, 6, v10\n\t"
    "v_lshl_add_u32 v2, v14, 4, v15\n\t"
    "v_add_u32 v2, 0x400, v2\n\t"
    "v_xor_b32 v3, 32, v2\n\t"
    "v_add_u32 v4, 0xfffffc80, v2\n\t"
    "v_add_u32 v5, 0xfffffc80, v3\n\t"
    "v_add_u32 v6, 0x1000, v2\n\t"
    "v_add_u32 v7, 0x1000, v3\n\t"
    "v_add_u32 v8, 0x1000, v4\n\t"
    "v_add_u32 v9, 0x1000, v5\n\t"
    "v_mov_b32 v38, 131072\n\t"
    "v_mov_b32 v23, 0\n\t"
    "s_cmp_lg_u32 s20, 0\n\t"
    "s_cbranch_scc1 .Lcomp%=\n\t"
    "s_memrealtime s[56:57]\n\t"
    "v_lshl_add_u32 v20, v1, 2, v38\n\t"
    "ds_write_b32 v20, v23\n\t"
    "v_bfe_u32 v24, v1, 2, 3\n\t"
    "v_lshrrev_b32 v25, 5, v1\n\t"
    "s_and_b32 s36, s24, 1\n\t"
    "v_lshlrev_b32 v24, 8, v24\n\t"
    "v_xor_b32 v25, s36, v25\n\t"
    "v_lshl_add_u32 v24, v25, 6, v24\n\t"
    "v_lshrrev_b32 v25, 3, v1\n\t"
    "v_and_b32 v25, 2, v25\n\t"
    "v_and_b32 v26, 3, v1\n\t"
    "v_xor_b32 v26, v25, v26\n\t"
    "s_lshr_b32 s36, s24, 1\n\t"
    "s_lshl_b32 s36, s36, 7\n\t"
    "v_lshl_add_u32 v24, v26, 4, v24\n\t"
    "s_add_u32 s37, s36, 0x400\n\t"
    "s_sub_u32 s36, 0x80, s36\n\t"
    "v_add_u32 v12, s37, v24\n\t"
    "v_add_u32 v13, s36, v24\n\t"
    "v_add_u32 v14, 0x1000, v12\n\t"
    "v_add_u32 v15, 0x1000, v13\n\t"
    "s_waitcnt lgkmcnt(0)\n\t"
    "s_add_u32 s28, s10, s25\n\t"
    "s_addc_u32 s29, s11, 0\n\t"
    "s_sub_u32 s28, s28, 0x400\n\t"
    "s_subb_u32 s29, s29, 0\n\t"
    "s_mov_b32 m0, 16384\n\t"
    "s_nop 0\n\t"
    "global_load_lds_dwordx4 v2, s[28:29] offset:0\n\t"
    "global_load_lds_dwordx4 v4, s[28:29] offset:1024\n\t"
    "global_load_lds_dwordx4 v3, s[28:29] offset:2048\n\t"
    "global_load_lds_dwordx4 v5, s[28:29] offset:3072\n\t"
    "s_add_u32 m0, m0, 0x1000\n\t"
    "s_nop 0\n\t"
    "global_load_lds_dwordx4 v6, s[28:29] offset:0\n\t"
    "global_load_lds_dwordx4 v8, s[28:29] offset:1024\n\t"
    "global_load_lds_dwordx4 v7, s[28:29] offset:2048\n\t"
    "global_load_lds_dwordx4 v9, s[28:29] offset:3072\n\t"
    "s_add_u32 s28, s28, 8192\n\t"
    "s_addc_u32 s29, s29, 0\n\t"
    "s_add_u32 s30, s12, s25\n\t"
    "s_addc_u32 s31, s13, 0\n\t"
    "s_sub_u32 s30, s30, 0x400\n\t"
    "s_subb_u32 s31, s31, 0\n\t"
    "s_barrier\n\t"
    "v_mov_b32 v21, -2\n\t"
    "v_mov_b32 v22, 0\n\t"
    "s_mov_b32 m0, 32768\n\t"
    "s_nop 0\n\t"
    "global_load_lds_dwordx4 v2, s[28:29] offset:0\n\t"
    "global_load_lds_dwordx4 v4, s[28:29] offset:1024\n\t"
    "global_load_lds_dwordx4 v3, s[28:29] offset:2048\n\t"
    "global_load_lds_dwordx4 v5, s[28:29] offset:3072\n\t"
    "s_add_u32 m0, m0, 0x1000\n\t"
    "s_nop 0\n\t"
    "global_load_lds_dwordx4 v6, s[28:29] offset:0\n\t"
    "global_load_lds_dwordx4 v8, s[28:29] offset:1024\n\t"
    "global_load_lds_dwordx4 v7, s[28:29] offset:2048\n\t"
    "global_load_lds_dwordx4 v9, s[28:29] offset:3072\n\t"
    "s_add_u32 s28, s28, 8192\n\t"
    "s_addc_u32 s29, s29, 0\n\t"
    "v_add_u32 v21, 1, v21\n\t"
    "s_waitcnt lgkmcnt(0)\n\t"
    "v_max_i32 v22, v22, v21\n\t"
    "s_waitcnt vmcnt(24)\n\t"
    "ds_write_b32 v38, v22\n\t"
    "s_mov_b32 m0, 40960\n\t"
    "s_nop 0\n\t"
    "global_load_lds_dwordx4 v12, s[30:31] offset:0\n\t"
    "global_load_lds_dwordx4 v13, s[30:31] offset:1024\n\t"
    "global_load_lds_dwordx4 v12, s[30:31] offset:2048\n\t"
    "global_load_lds_dwordx4 v13, s[30:31] offset:3072\n\t"
    "s_add_u32 m0, m0, 0x1000\n\t"
    "s_nop 0\n\t"
    "global_load_lds_dwordx4 v14, s[30:31] offset:0\n\t"
    "global_load_lds_dwordx4 v15, s[30:31] offset:1024\n\t"
    "global_load_lds_dwordx4 v14, s[30:31] offset:2048\n\t"
    "global_load_lds_dwordx4 v15, s[30:31] offset:3072\n\t"
    "s_add_u32 s30, s30, 8192\n\t"
    "s_addc_u32 s31, s31, 0\n\t"
    "v_add_u32 v21, 1, v21\n\t"
    "s_waitcnt lgkmcnt(0)\n\t"
    "v_max_i32 v22, v22, v21\n\t"
    "s_waitcnt vmcnt(24)\n\t"
    "ds_write_b32 v38, v22\n\t"
    "s_add_u32 s56, s56, -60\n\t"
    "s_lshr_b32 s36, s26, 1\n\t"
    "s_mul_i32 s36, s36, 11\n\t"
    "s_and_b32 s36, s36, 15\n\t"
    "s_add_u32 s56, s56, s36\n\t"
    "s_mov_b32 s35, 2\n\t"
    "s_mov_b32 s33, 49152\n\t"
    "s_mov_b32 s34, 0\n\t"
    ".Lld%=:\n\t"
    "s_cmp_lt_u32 s35, 6\n\t"
    "s_cbranch_scc1 .Lldgo%=\n\t"
    "s_sub_u32 s51, s35, 7\n\t"
    "s_max_i32 s51, s51, 2\n\t"
    ".Lldp%=:\n\t"
    "s_cmp_ge_i32 s34, s51\n\t"
    "s_cbranch_scc1 .Lldgo%=\n\t"
    "ds_read_b128 v[24:27], v38 offset:16\n\t"
    "s_waitcnt lgkmcnt(0)\n\t"
    "v_min3_u32 v24, v24, v25, v26\n\t"
    "v_min_u32 v24, v24, v27\n\t"
    "s_nop 0\n\t"
    "v_readfirstlane_b32 s34, v24\n\t"
    "s_cmp_ge_i32 s34, s51\n\t"
    "s_cbranch_scc1 .Lldgo%=\n\t"
    "s_lshl_b32 s36, s35, 1\n\t"
    "s_sub_u32 s36, s36, 1\n\t"
    "v_mov_b32 v22, s36\n\t"
    "s_waitcnt vmcnt(0)\n\t"
    "ds_write_b32 v38, v22\n\t"
    "s_sleep 2\n\t"
    "s_branch .Lldp%=\n\t"
    ".Lldgo%=:\n\t"
    "s_add_u32 s36, s33, 0x2000\n\t"
    "s_cmp_eq_u32 s35, 16\n\t"
    "s_cbranch_scc1 .Lldlast%=\n\t"
    "s_cmp_ge_u32 s35, 9\n\t"
    "s_cbranch_scc1 .Luk%=\n\t"
    "s_add_u32 s56, s56, 34\n\t"
    ".Ltk%=:\n\t"
    "s_memrealtime s[58:59]\n\t"
    "s_waitcnt lgkmcnt(0)\n\t"
    "s_sub_u32 s57, s58, s56\n\t"
    "s_cmp_ge_i32 s57, 0\n\t"
    "s_cbranch_scc1 .Luk%=\n\t"
    "s_branch .Ltk%=\n\t"
    ".Luk%=:\n\t"
    "s_mov_b32 m0, s33\n\t"
    "s_nop 0\n\t"
    "global_load_lds_dwordx4 v2, s[28:29] offset:0\n\t"
    "global_load_lds_dwordx4 v4, s[28:29] offset:1024\n\t"
    "global_load_lds_dwordx4 v3, s[28:29] offset:2048\n\t"
    "global_load_lds_dwordx4 v5, s[28:29] offset:3072\n\t"
    "s_add_u32 m0, m0, 0x1000\n\t"
    "s_nop 0\n\t"
    "global_load_lds_dwordx4 v6, s[28:29] offset:0\n\t"
    "global_load_lds_dwordx4 v8, s[28:29] offset:1024\n\t"
    "global_load_lds_dwordx4 v7, s[28:29] offset:2048\n\t"
    "global_load_lds_dwordx4 v9, s[28:29] offset:3072\n\t"
    "s_add_u32 s28, s28, 8192\n\t"
    "s_addc_u32 s29, s29, 0\n\t"
    "v_add_u32 v21, 1, v21\n\t"
    "s_waitcnt lgkmcnt(0)\n\t"
    "v_max_i32 v22, v22, v21\n\t"
    "s_waitcnt vmcnt(24)\n\t"
    "ds_write_b32 v38, v22\n\t"
    "s_cmp_ge_u32 s35, 9\n\t"
    "s_cbranch_scc1 .Luv%=\n\t"
    "s_add_u32 s56, s56, 34\n\t"
    ".Ltv%=:\n\t"
    "s_memrealtime s[58:59]\n\t"
    "s_waitcnt lgkmcnt(0)\n\t"
    "s_sub_u32 s57, s58, s56\n\t"
    "s_cmp_ge_i32 s57, 0\n\t"
    "s_cbranch_scc1 .Luv%=\n\t"
    "s_branch .Ltv%=\n\t"
    ".Luv%=:\n\t"
    "s_mov_b32 m0, s36\n\t"
    "s_nop 0\n\t"
    "global_load_lds_dwordx4 v12, s[30:31] offset:0\n\t"
    "global_load_lds_dwordx4 v13, s[30:31] offset:1024\n\t"
    "global_load_lds_dwordx4 v12, s[30:31] offset:2048\n\t"
    "global_load_lds_dwordx4 v13, s[30:31] offset:3072\n\t"
    "s_add_u32 m0, m0, 0x1000\n\t"
    "s_nop 0\n\t"
    "global_load_lds_dwordx4 v14, s[30:31] offset:0\n\t"
    "global_load_lds_dwordx4 v15, s[30:31] offset:1024\n\t"
    "global_load_lds_dwordx4 v14, s[30:31] offset:2048\n\t"
    "global_load_lds_dwordx4 v15, s[30:31] offset:3072\n\t"
    "s_add_u32 s30, s30, 8192\n\t"
    "s_addc_u32 s31, s31, 0\n\t"
    "v_add_u32 v21, 1, v21\n\t"
    "s_waitcnt lgkmcnt(0)\n\t"
    "v_max_i32 v22, v22, v21\n\t"
    "s_waitcnt vmcnt(24)\n\t"
    "ds_write_b32 v38, v22\n\t"
    "s_add_u32 s35, s35, 1\n\t"
    "s_add_u32 s33, s33, 16384\n\t"
    "s_and_b32 s33, s33, 0x1ffff\n\t"
    "s_branch .Lld%=\n\t"
    ".Lldlast%=:\n\t"
    "s_mov_b32 m0, s36\n\t"
    "s_nop 0\n\t"
    "global_load_lds_dwordx4 v12, s[30:31] offset:0\n\t"
    "global_load_lds_dwordx4 v13, s[30:31] offset:1024\n\t"
    "global_load_lds_dwordx4 v12, s[30:31] offset:2048\n\t"
    "global_load_lds_dwordx4 v13, s[30:31] offset:3072\n\t"
    "s_add_u32 m0, m0, 0x1000\n\t"
    "s_nop 0\n\t"
    "global_load_lds_dwordx4 v14, s[30:31] offset:0\n\t"
    "global_load_lds_dwordx4 v15, s[30:31] offset:1024\n\t"
    "global_load_lds_dwordx4 v14, s[30:31] offset:2048\n\t"
    "global_load_lds_dwordx4 v15, s[30:31] offset:3072\n\t"
    "v_add_u32 v21, 1, v21\n\t"
    "s_waitcnt lgkmcnt(0)\n\t"
    "v_max_i32 v22, v22, v21\n\t"
    "s_waitcnt vmcnt(24)\n\t"
    "ds_write_b32 v38, v22\n\t"
    "v_add_u32 v21, 1, v21\n\t"
    "s_waitcnt lgkmcnt(0)\n\t"
    "v_max_i32 v22, v22, v21\n\t"
    "s_waitcnt vmcnt(16)\n\t"
    "ds_write_b32 v38, v22\n\t"
    "v_add_u32 v21, 1, v21\n\t"
    "s_waitcnt lgkmcnt(0)\n\t"
    "v_max_i32 v22, v22, v21\n\t"
    "s_waitcnt vmcnt(8)\n\t"
    "ds_write_b32 v38, v22\n\t"
    "v_mov_b32 v21, 33\n\t"
    "s_waitcnt vmcnt(0)\n\t"
    "ds_write_b32 v38, v21\n\t"
    "s_lshl_b32 s41, s21, 1\n\t"
    "s_add_u32 s41, s41, s23\n\t"
    "s_mul_i32 s44, s41, 147456\n\t"
    "s_add_u32 s44, s44, 33792\n\t"
    "s_lshl_b32 s36, s22, 7\n\t"
    "s_add_u32 s44, s44, s36\n\t"
    "v_mul_u32_u24 v24, 36864, v1\n\t"
    "v_add_u32 v24, s44, v24\n\t"
    "s_mov_b64 exec, 15\n\t"
    "global_load_dword v16, v24, s[16:17] sc1\n\t"
    "s_waitcnt vmcnt(0)\n\t"
    "s_endpgm\n\t"
    ".Lhelp%=:\n\t"
    "s_lshl_b32 s41, s21, 1\n\t"
    "s_add_u32 s41, s41, s23\n\t"
    "s_mul_i32 s44, s41, 147456\n\t"
    "s_lshl_b32 s42, s41, 15\n\t"
    "s_waitcnt lgkmcnt(0)\n\t"
    "s_add_u32 s12, s12, s25\n\t"
    "s_addc_u32 s13, s13, 0\n\t"
    "s_add_u32 s10, s10, s25\n\t"
    "s_addc_u32 s11, s11, 0\n\t"
    "s_add_u32 s16, s16, s44\n\t"
    "s_addc_u32 s17, s17, 0\n\t"
    "s_add_u32 s14, s14, s42\n\t"
    "s_addc_u32 s15, s15, 0\n\t"
    "s_load_dword s40, s[12:13], 0\n\t"
    "s_load_dword s41, s[12:13], 32768\n\t"
    "s_load_dword s42, s[10:11], 32768\n\t"
    "s_barrier\n\t"
    "s_load_dword s43, s[16:17], 0\n\t"
    "s_load_dword s44, s[14:15], 0\n\t"
    "s_load_dword s45, s[12:13], 65536\n\t"
    "s_load_dword s46, s[10:11], 65536\n\t"
    "s_load_dword s47, s[12:13], 98304\n\t"
    "s_load_dword s48, s[10:11], 98304\n\t"
    "s_load_dword s49, s[16:17], 30720\n\t"
    "s_load_dword s50, s[16:17], 57344\n\t"
    "s_load_dword s51, s[16:17], 83968\n\t"
    "s_waitcnt lgkmcnt(0)\n\t"
    "s_load_dword s40, s[16:17], 109568\n\t"
    "s_load_dword s41, s[16:17], 141312\n\t"
    "s_load_dword s42, s[16:17], 146432\n\t"
    "s_endpgm\n\t"
    ".Lcomp%=:\n\t"
    "s_sub_u32 s40, s20, 1\n\t"
    "s_lshl_b32 s41, s21, 3\n\t"
    "s_lshl_b32 s36, s23, 2\n\t"
    "s_add_u32 s41, s41, s36\n\t"
    "s_add_u32 s41, s41, s40\n\t"
    "s_lshl_b32 s42, s41, 13\n\t"
    "s_lshl_b32 s43, s40, 13\n\t"
    "s_add_u32 s43, s43, 114688\n\t"
    "s_and_b32 s43, s43, 0x1ffff\n\t"
    "s_mul_i32 s44, s41, 36864\n\t"
    "v_and_b32 v24, 15, v1\n\t"
    "v_lshrrev_b32 v25, 4, v1\n\t"
    "v_lshrrev_b32 v26, 2, v24\n\t"
    "v_sub_u32 v26, 0, v26\n\t"
    "v_and_b32 v26, 3, v26\n\t"
    "v_xor_b32 v26, v26, v25\n\t"
    "v_lshlrev_b32 v26, 4, v26\n\t"
    "v_lshrrev_b32 v27, 3, v24\n\t"
    "v_lshl_add_u32 v26, v27, 11, v26\n\t"
    "v_and_b32 v27, 7, v24\n\t"
    "v_lshl_add_u32 v29, v27, 6, v26\n\t"
    "v_lshlrev_b32 v26, 11, v11\n\t"
    "v_mul_u32_u24 v27, 288, v12\n\t"
    "v_add_u32 v26, v26, v27\n\t"
    "v_bfe_u32 v27, v1, 2, 2\n\t"
    "v_lshl_add_u32 v26, v27, 6, v26\n\t"
    "v_lshl_add_u32 v30, v13, 3, v26\n\t"
    "v_xor_b32 v31, 32, v30\n\t"
    "s_lshl_b32 s36, s22, 6\n\t"
    "v_lshlrev_b32 v26, 8, v24\n\t"
    "v_lshl_add_u32 v26, v12, 5, v26\n\t"
    "v_lshl_add_u32 v26, v11, 4, v26\n\t"
    "v_add_u32 v22, s36, v26\n\t"
    "v_lshlrev_b32 v11, 4, v24\n\t"
    "v_add_u32 v11, 32768, v11\n\t"
    "s_lshl_b32 s36, s22, 8\n\t"
    "v_lshl_add_u32 v26, v25, 2, v11\n\t"
    "v_add_u32 v18, s36, v26\n\t"
    "v_xor_b32 v26, s24, v1\n\t"
    "v_and_b32 v26, 3, v26\n\t"
    "v_lshlrev_b32 v26, 7, v26\n\t"
    "v_add_u32 v26, 33792, v26\n\t"
    "s_waitcnt lgkmcnt(0)\n\t"
    "s_add_u32 s28, s8, s42\n\t"
    "s_addc_u32 s29, s9, 0\n\t"
    "s_sub_u32 s28, s28, 0x400\n\t"
    "s_subb_u32 s29, s29, 0\n\t"
    "s_mov_b32 m0, s43\n\t"
    "s_nop 0\n\t"
    "global_load_lds_dwordx4 v2, s[28:29] offset:0\n\t"
    "global_load_lds_dwordx4 v4, s[28:29] offset:1024\n\t"
    "global_load_lds_dwordx4 v3, s[28:29] offset:2048\n\t"
    "global_load_lds_dwordx4 v5, s[28:29] offset:3072\n\t"
    "s_add_u32 m0, m0, 0x1000\n\t"
    "s_nop 0\n\t"
    "global_load_lds_dwordx4 v6, s[28:29] offset:0\n\t"
    "global_load_lds_dwordx4 v8, s[28:29] offset:1024\n\t"
    "global_load_lds_dwordx4 v7, s[28:29] offset:2048\n\t"
    "global_load_lds_dwordx4 v9, s[28:29] offset:3072\n\t"
    "s_barrier\n\t"
    "s_add_u32 s16, s16, s44\n\t"
    "s_addc_u32 s17, s17, 0\n\t"
    "s_add_u32 s14, s14, s42\n\t"
    "s_addc_u32 s15, s15, 0\n\t"
    "s_and_b32 s36, s26, 7\n\t"
    "s_lshl_b32 s36, s36, 2\n\t"
    "s_add_u32 s36, s36, s40\n\t"
    "s_lshl_b32 s36, s36, 8\n\t"
    "v_lshlrev_b32 v25, 7, v1\n\t"
    "v_add_u32 v25, s36, v25\n\t"
    "v_min_u32 v25, .Lslow%=-.Lbase%=, v25\n\t"
    "s_mov_b64 exec, 3\n\t"
    "global_load_dword v27, v25, s[38:39]\n\t"
    "s_mov_b64 exec, -1\n\t"
    "v_lshlrev_b32 v24, 4, v1\n\t"
    "s_xor_b32 s36, s24, 0\n\t"
    "s_lshl_b32 s37, s36, 2\n\t"
    "s_add_u32 s37, s37, s22\n\t"
    "s_lshl_b32 s37, s37, 11\n\t"
    "v_add_u32 v12, s37, v24\n\t"
    "s_lshl_b32 s37, s22, 2\n\t"
    "s_add_u32 s37, s37, s36\n\t"
    "s_lshl_b32 s37, s37, 11\n\t"
    "v_add_u32 v15, s37, v24\n\t"
    "s_lshl_b32 s37, s36, 8\n\t"
    "v_add_u32 v19, s37, v11\n\t"
    "s_xor_b32 s36, s24, 1\n\t"
    "s_lshl_b32 s37, s36, 2\n\t"
    "s_add_u32 s37, s37, s22\n\t"
    "s_lshl_b32 s37, s37, 11\n\t"
    "v_add_u32 v13, s37, v24\n\t"
    "s_lshl_b32 s37, s22, 2\n\t"
    "s_add_u32 s37, s37, s36\n\t"
    "s_lshl_b32 s37, s37, 11\n\t"
    "v_add_u32 v16, s37, v24\n\t"
    "s_lshl_b32 s37, s36, 8\n\t"
    "v_add_u32 v20, s37, v11\n\t"
    "s_xor_b32 s36, s24, 2\n\t"
    "s_lshl_b32 s37, s36, 2\n\t"
    "s_add_u32 s37, s37, s22\n\t"
    "s_lshl_b32 s37, s37, 11\n\t"
    "v_add_u32 v14, s37, v24\n\t"
    "s_lshl_b32 s37, s22, 2\n\t"
    "s_add_u32 s37, s37, s36\n\t"
    "s_lshl_b32 s37, s37, 11\n\t"
    "v_add_u32 v17, s37, v24\n\t"
    "s_lshl_b32 s37, s36, 8\n\t"
    "v_add_u32 v21, s37, v11\n\t"
    "s_lshl_b32 s36, s40, 2\n\t"
    "s_add_u32 s36, s36, 131088\n\t"
    "v_mov_b32 v28, s36\n\t"
    "v_mov_b32 v39, 2\n\t"
    "v_add_u32 v32, s43, v29\n\t"
    "v_mov_b32 v33, v30\n\t"
    "v_mov_b32 v34, v31\n\t"
    "s_mov_b32 s46, 0x3e0293ee\n\t"
    "s_mov_b32 s47, 0x3e0293ee\n\t"
    "s_mov_b32 s45, 0xffff0000\n\t"
    "s_setprio 2\n\t"
    "v_mov_b64 v[192:193], 0\n\t"
    "v_mov_b64 v[194:195], 0\n\t"
    "v_mov_b64 v[196:197], 0\n\t"
    "v_mov_b64 v[198:199], 0\n\t"
    "v_mov_b64 v[200:201], 0\n\t"
    "v_mov_b64 v[202:203], 0\n\t"
    "v_mov_b64 v[204:205], 0\n\t"
    "v_mov_b64 v[206:207], 0\n\t"
    "v_mov_b64 v[208:209], 0\n\t"
    "v_mov_b64 v[210:211], 0\n\t"
    "v_mov_b64 v[212:213], 0\n\t"
    "v_mov_b64 v[214:215], 0\n\t"
    "v_mov_b64 v[216:217], 0\n\t"
    "v_mov_b64 v[218:219], 0\n\t"
    "v_mov_b64 v[220:221], 0\n\t"
    "v_mov_b64 v[222:223], 0\n\t"
    "v_mov_b64 v[224:225], 0\n\t"
    "v_mov_b64 v[226:227], 0\n\t"
    "v_mov_b64 v[228:229], 0\n\t"
    "v_mov_b64 v[230:231], 0\n\t"
    "v_mov_b64 v[232:233], 0\n\t"
    "v_mov_b64 v[234:235], 0\n\t"
    "v_mov_b64 v[236:237], 0\n\t"
    "v_mov_b64 v[238:239], 0\n\t"
    "v_mov_b64 v[240:241], 0\n\t"
    "v_mov_b64 v[242:243], 0\n\t"
    "v_mov_b64 v[244:245], 0\n\t"
    "v_mov_b64 v[246:247], 0\n\t"
    "v_mov_b64 v[248:249], 0\n\t"
    "v_mov_b64 v[250:251], 0\n\t"
    "v_mov_b64 v[252:253], 0\n\t"
    "v_mov_b64 v[254:255], 0\n\t"
    "s_waitcnt vmcnt(1)\n\t"
    "ds_read_b128 v[160:163], v32 offset:0\n\t"
    "ds_read_b128 v[164:167], v32 offset:512\n\t"
    "ds_read_b128 v[168:171], v32 offset:1024\n\t"
    "ds_read_b128 v[172:175], v32 offset:1536\n\t"
    "ds_read_b128 v[176:179], v32 offset:4096\n\t"
    "ds_read_b128 v[180:183], v32 offset:4608\n\t"
    "ds_read_b128 v[184:187], v32 offset:5120\n\t"
    "ds_read_b128 v[188:191], v32 offset:5632\n\t"
    "v_mov_b32 v32, v29\n\t"
    "s_waitcnt lgkmcnt(6)\n\t"
    "v_lshlrev_b32 v40, 16, v160\n\t"
    "v_and_b32 v41, s45, v160\n\t"
    "v_lshlrev_b32 v42, 16, v161\n\t"
    "v_and_b32 v43, s45, v161\n\t"
    "v_lshlrev_b32 v44, 16, v162\n\t"
    "v_and_b32 v45, s45, v162\n\t"
    "v_lshlrev_b32 v46, 16, v163\n\t"
    "v_and_b32 v47, s45, v163\n\t"
    "v_pk_mul_f32 v[40:41], v[40:41], s[46:47]\n\t"
    "v_pk_mul_f32 v[42:43], v[42:43], s[46:47]\n\t"
    "v_pk_mul_f32 v[44:45], v[44:45], s[46:47]\n\t"
    "v_pk_mul_f32 v[46:47], v[46:47], s[46:47]\n\t"
    "v_cvt_pk_bf16_f32 v160, v40, v41\n\t"
    "v_cvt_pk_bf16_f32 v161, v42, v43\n\t"
    "v_cvt_pk_bf16_f32 v162, v44, v45\n\t"
    "v_cvt_pk_bf16_f32 v163, v46, v47\n\t"
    "v_lshlrev_b32 v48, 16, v164\n\t"
    "v_and_b32 v49, s45, v164\n\t"
    "v_lshlrev_b32 v50, 16, v165\n\t"
    "v_and_b32 v51, s45, v165\n\t"
    "v_lshlrev_b32 v52, 16, v166\n\t"
    "v_and_b32 v53, s45, v166\n\t"
    "v_lshlrev_b32 v54, 16, v167\n\t"
    "v_and_b32 v55, s45, v167\n\t"
    "v_pk_mul_f32 v[48:49], v[48:49], s[46:47]\n\t"
    "v_pk_mul_f32 v[50:51], v[50:51], s[46:47]\n\t"
    "v_pk_mul_f32 v[52:53], v[52:53], s[46:47]\n\t"
    "v_pk_mul_f32 v[54:55], v[54:55], s[46:47]\n\t"
    "v_cvt_pk_bf16_f32 v164, v48, v49\n\t"
    "v_cvt_pk_bf16_f32 v165, v50, v51\n\t"
    "v_cvt_pk_bf16_f32 v166, v52, v53\n\t"
    "v_cvt_pk_bf16_f32 v167, v54, v55\n\t"
    "s_waitcnt lgkmcnt(4)\n\t"
    "v_lshlrev_b32 v40, 16, v168\n\t"
    "v_and_b32 v41, s45, v168\n\t"
    "v_lshlrev_b32 v42, 16, v169\n\t"
    "v_and_b32 v43, s45, v169\n\t"
    "v_lshlrev_b32 v44, 16, v170\n\t"
    "v_and_b32 v45, s45, v170\n\t"
    "v_lshlrev_b32 v46, 16, v171\n\t"
    "v_and_b32 v47, s45, v171\n\t"
    "v_pk_mul_f32 v[40:41], v[40:41], s[46:47]\n\t"
    "v_pk_mul_f32 v[42:43], v[42:43], s[46:47]\n\t"
    "v_pk_mul_f32 v[44:45], v[44:45], s[46:47]\n\t"
    "v_pk_mul_f32 v[46:47], v[46:47], s[46:47]\n\t"
    "v_cvt_pk_bf16_f32 v168, v40, v41\n\t"
    "v_cvt_pk_bf16_f32 v169, v42, v43\n\t"
    "v_cvt_pk_bf16_f32 v170, v44, v45\n\t"
    "v_cvt_pk_bf16_f32 v171, v46, v47\n\t"
    "v_lshlrev_b32 v48, 16, v172\n\t"
    "v_and_b32 v49, s45, v172\n\t"
    "v_lshlrev_b32 v50, 16, v173\n\t"
    "v_and_b32 v51, s45, v173\n\t"
    "v_lshlrev_b32 v52, 16, v174\n\t"
    "v_and_b32 v53, s45, v174\n\t"
    "v_lshlrev_b32 v54, 16, v175\n\t"
    "v_and_b32 v55, s45, v175\n\t"
    "v_pk_mul_f32 v[48:49], v[48:49], s[46:47]\n\t"
    "v_pk_mul_f32 v[50:51], v[50:51], s[46:47]\n\t"
    "v_pk_mul_f32 v[52:53], v[52:53], s[46:47]\n\t"
    "v_pk_mul_f32 v[54:55], v[54:55], s[46:47]\n\t"
    "v_cvt_pk_bf16_f32 v172, v48, v49\n\t"
    "v_cvt_pk_bf16_f32 v173, v50, v51\n\t"
    "v_cvt_pk_bf16_f32 v174, v52, v53\n\t"
    "v_cvt_pk_bf16_f32 v175, v54, v55\n\t"
    "s_waitcnt lgkmcnt(0)\n\t"
    "v_lshlrev_b32 v40, 16, v176\n\t"
    "v_and_b32 v41, s45, v176\n\t"
    "v_lshlrev_b32 v42, 16, v177\n\t"
    "v_and_b32 v43, s45, v177\n\t"
    "v_lshlrev_b32 v44, 16, v178\n\t"
    "v_and_b32 v45, s45, v178\n\t"
    "v_lshlrev_b32 v46, 16, v179\n\t"
    "v_and_b32 v47, s45, v179\n\t"
    "v_pk_mul_f32 v[40:41], v[40:41], s[46:47]\n\t"
    "v_pk_mul_f32 v[42:43], v[42:43], s[46:47]\n\t"
    "v_pk_mul_f32 v[44:45], v[44:45], s[46:47]\n\t"
    "v_pk_mul_f32 v[46:47], v[46:47], s[46:47]\n\t"
    "v_cvt_pk_bf16_f32 v176, v40, v41\n\t"
    "v_cvt_pk_bf16_f32 v177, v42, v43\n\t"
    "v_cvt_pk_bf16_f32 v178, v44, v45\n\t"
    "v_cvt_pk_bf16_f32 v179, v46, v47\n\t"
    "v_lshlrev_b32 v48, 16, v180\n\t"
    "v_and_b32 v49, s45, v180\n\t"
    "v_lshlrev_b32 v50, 16, v181\n\t"
    "v_and_b32 v51, s45, v181\n\t"
    "v_lshlrev_b32 v52, 16, v182\n\t"
    "v_and_b32 v53, s45, v182\n\t"
    "v_lshlrev_b32 v54, 16, v183\n\t"
    "v_and_b32 v55, s45, v183\n\t"
    "v_pk_mul_f32 v[48:49], v[48:49], s[46:47]\n\t"
    "v_pk_mul_f32 v[50:51], v[50:51], s[46:47]\n\t"
    "v_pk_mul_f32 v[52:53], v[52:53], s[46:47]\n\t"
    "v_pk_mul_f32 v[54:55], v[54:55], s[46:47]\n\t"
    "v_cvt_pk_bf16_f32 v180, v48, v49\n\t"
    "v_cvt_pk_bf16_f32 v181, v50, v51\n\t"
    "v_cvt_pk_bf16_f32 v182, v52, v53\n\t"
    "v_cvt_pk_bf16_f32 v183, v54, v55\n\t"
    "v_lshlrev_b32 v40, 16, v184\n\t"
    "v_and_b32 v41, s45, v184\n\t"
    "v_lshlrev_b32 v42, 16, v185\n\t"
    "v_and_b32 v43, s45, v185\n\t"
    "v_lshlrev_b32 v44, 16, v186\n\t"
    "v_and_b32 v45, s45, v186\n\t"
    "v_lshlrev_b32 v46, 16, v187\n\t"
    "v_and_b32 v47, s45, v187\n\t"
    "v_pk_mul_f32 v[40:41], v[40:41], s[46:47]\n\t"
    "v_pk_mul_f32 v[42:43], v[42:43], s[46:47]\n\t"
    "v_pk_mul_f32 v[44:45], v[44:45], s[46:47]\n\t"
    "v_pk_mul_f32 v[46:47], v[46:47], s[46:47]\n\t"
    "v_cvt_pk_bf16_f32 v184, v40, v41\n\t"
    "v_cvt_pk_bf16_f32 v185, v42, v43\n\t"
    "v_cvt_pk_bf16_f32 v186, v44, v45\n\t"
    "v_cvt_pk_bf16_f32 v187, v46, v47\n\t"
    "v_lshlrev_b32 v48, 16, v188\n\t"
    "v_and_b32 v49, s45, v188\n\t"
    "v_lshlrev_b32 v50, 16, v189\n\t"
    "v_and_b32 v51, s45, v189\n\t"
    "v_lshlrev_b32 v52, 16, v190\n\t"
    "v_and_b32 v53, s45, v190\n\t"
    "v_lshlrev_b32 v54, 16, v191\n\t"
    "v_and_b32 v55, s45, v191\n\t"
    "v_pk_mul_f32 v[48:49], v[48:49], s[46:47]\n\t"
    "v_pk_mul_f32 v[50:51], v[50:51], s[46:47]\n\t"
    "v_pk_mul_f32 v[52:53], v[52:53], s[46:47]\n\t"
    "v_pk_mul_f32 v[54:55], v[54:55], s[46:47]\n\t"
    "v_cvt_pk_bf16_f32 v188, v48, v49\n\t"
    "v_cvt_pk_bf16_f32 v189, v50, v51\n\t"
    "v_cvt_pk_bf16_f32 v190, v52, v53\n\t"
    "v_cvt_pk_bf16_f32 v191, v54, v55\n\t"
    ".Lpa%=:\n\t"
    "ds_read_b32 v37, v38\n\t"
    "s_waitcnt lgkmcnt(0)\n\t"
    "v_readfirstlane_b32 s34, v37\n\t"
    "s_cmp_ge_i32 s34, 2\n\t"
    "s_cbranch_scc1 .Lqa%=\n\t"
    "s_sleep 1\n\t"
    "s_branch .Lpa%=\n\t"
    ".Lqa%=:\n\t"
    "ds_read_b128 v[128:131], v32 offset:16384\n\t"
    "ds_read_b128 v[132:135], v32 offset:16896\n\t"
    "ds_read_b128 v[136:139], v32 offset:17408\n\t"
    "ds_read_b128 v[140:143], v32 offset:17920\n\t"
    "ds_read_b128 v[144:147], v32 offset:20480\n\t"
    "ds_read_b128 v[148:151], v32 offset:20992\n\t"
    "ds_read_b128 v[152:155], v32 offset:21504\n\t"
    "ds_read_b128 v[156:159], v32 offset:22016\n\t"
    "ds_read_b128 v[96:99], v32 offset:32768\n\t"
    "ds_read_b128 v[100:103], v32 offset:33280\n\t"
    "ds_read_b128 v[104:107], v32 offset:33792\n\t"
    "ds_read_b128 v[108:111], v32 offset:34304\n\t"
    "ds_read_b128 v[112:115], v32 offset:36864\n\t"
    "ds_read_b128 v[116:119], v32 offset:37376\n\t"
    "ds_read_b128 v[120:123], v32 offset:37888\n\t"
    "ds_read_b128 v[124:127], v32 offset:38400\n\t"
    "ds_read_b32 v37, v38\n\t"
    "s_waitcnt lgkmcnt(15)\n\t"
    "v_mfma_f32_16x16x32_bf16 v[64:67], v[128:131], v[160:163], 0\n\t"
    "v_mfma_f32_16x16x32_bf16 v[68:71], v[128:131], v[176:179], 0\n\t"
    "v_mfma_f32_16x16x32_bf16 v[64:67], v[132:135], v[164:167], v[64:67]\n\t"
    "v_mfma_f32_16x16x32_bf16 v[68:71], v[132:135], v[180:183], v[68:71]\n\t"
    "s_waitcnt lgkmcnt(13)\n\t"
    "v_mfma_f32_16x16x32_bf16 v[64:67], v[136:139], v[168:171], v[64:67]\n\t"
    "v_mfma_f32_16x16x32_bf16 v[68:71], v[136:139], v[184:187], v[68:71]\n\t"
    "v_mfma_f32_16x16x32_bf16 v[64:67], v[140:143], v[172:175], v[64:67]\n\t"
    "v_mfma_f32_16x16x32_bf16 v[68:71], v[140:143], v[188:191], v[68:71]\n\t"
    "s_waitcnt lgkmcnt(11)\n\t"
    "v_mfma_f32_16x16x32_bf16 v[72:75], v[144:147], v[160:163], 0\n\t"
    "v_mfma_f32_16x16x32_bf16 v[76:79], v[144:147], v[176:179], 0\n\t"
    "v_mfma_f32_16x16x32_bf16 v[72:75], v[148:151], v[164:167], v[72:75]\n\t"
    "v_mfma_f32_16x16x32_bf16 v[76:79], v[148:151], v[180:183], v[76:79]\n\t"
    "s_waitcnt lgkmcnt(9)\n\t"
    "v_mfma_f32_16x16x32_bf16 v[72:75], v[152:155], v[168:171], v[72:75]\n\t"
    "v_mfma_f32_16x16x32_bf16 v[76:79], v[152:155], v[184:187], v[76:79]\n\t"
    "v_mfma_f32_16x16x32_bf16 v[72:75], v[156:159], v[172:175], v[72:75]\n\t"
    "v_mfma_f32_16x16x32_bf16 v[76:79], v[156:159], v[188:191], v[76:79]\n\t"
    "s_waitcnt lgkmcnt(0)\n\t"
    "v_readfirstlane_b32 s34, v37\n\t"
    "v_mfma_f32_16x16x32_bf16 v[80:83], v[96:99], v[160:163], 0\n\t"
    "s_cmp_ge_i32 s34, 3\n\t"
    "s_cbranch_scc1 .Lqb%=\n\t"
    ".Lpb%=:\n\t"
    "ds_read_b32 v37, v38\n\t"
    "s_waitcnt lgkmcnt(0)\n\t"
    "v_readfirstlane_b32 s34, v37\n\t"
    "s_cmp_ge_i32 s34, 3\n\t"
    "s_cbranch_scc1 .Lqb%=\n\t"
    "s_sleep 1\n\t"
    "s_branch .Lpb%=\n\t"
    ".Lqb%=:\n\t"
    "v_mfma_f32_16x16x32_bf16 v[84:87], v[96:99], v[176:179], 0\n\t"
    "ds_read_b64_tr_b16 v[96:97], v33 offset:40960\n\t"
    "ds_read_b64_tr_b16 v[98:99], v33 offset:45056\n\t"
    "v_mfma_f32_16x16x32_bf16 v[80:83], v[100:103], v[164:167], v[80:83]\n\t"
    "v_max3_f32 v24, v64, v65, v66\n\t"
    "v_max3_f32 v40, v68, v69, v70\n\t"
    "v_max3_f32 v25, v67, v72, v73\n\t"
    "v_mfma_f32_16x16x32_bf16 v[84:87], v[100:103], v[180:183], v[84:87]\n\t"
    "ds_read_b64_tr_b16 v[100:101], v34 offset:40960\n\t"
    "ds_read_b64_tr_b16 v[102:103], v34 offset:45056\n\t"
    "v_max3_f32 v41, v71, v76, v77\n\t"
    "v_max3_f32 v24, v24, v74, v75\n\t"
    "v_max3_f32 v40, v40, v78, v79\n\t"
    "v_mfma_f32_16x16x32_bf16 v[80:83], v[104:107], v[168:171], v[80:83]\n\t"
    "v_max_f32 v24, v24, v25\n\t"
    "v_max_f32 v40, v40, v41\n\t"
    "v_mov_b32 v25, v24\n\t"
    "v_mfma_f32_16x16x32_bf16 v[84:87], v[104:107], v[184:187], v[84:87]\n\t"
    "ds_read_b64_tr_b16 v[104:105], v33 offset:41472\n\t"
    "ds_read_b64_tr_b16 v[106:107], v33 offset:45568\n\t"
    "v_mov_b32 v41, v40\n\t"
    "s_nop 0\n\t"
    "v_permlane16_swap_b32 v24, v25\n\t"
    "v_mfma_f32_16x16x32_bf16 v[80:83], v[108:111], v[172:175], v[80:83]\n\t"
    "v_permlane16_swap_b32 v40, v41\n\t"
    "s_nop 0\n\t"
    "v_max_f32 v24, v24, v25\n\t"
    "v_mfma_f32_16x16x32_bf16 v[84:87], v[108:111], v[188:191], v[84:87]\n\t"
    "ds_read_b64_tr_b16 v[108:109], v34 offset:41472\n\t"
    "ds_read_b64_tr_b16 v[110:111], v34 offset:45568\n\t"
    "v_max_f32 v40, v40, v41\n\t"
    "v_mov_b32 v25, v24\n\t"
    "v_mov_b32 v41, v40\n\t"
    "v_mfma_f32_16x16x32_bf16 v[88:91], v[112:115], v[160:163], 0\n\t"
    "s_nop 0\n\t"
    "v_permlane32_swap_b32 v24, v25\n\t"
    "v_permlane32_swap_b32 v40, v41\n\t"
    "v_mfma_f32_16x16x32_bf16 v[92:95], v[112:115], v[176:179], 0\n\t"
    "ds_read_b64_tr_b16 v[112:113], v33 offset:41984\n\t"
    "ds_read_b64_tr_b16 v[114:115], v33 offset:46080\n\t"
    "s_nop 0\n\t"
    "v_max_f32 v35, v24, v25\n\t"
    "v_max_f32 v36, v40, v41\n\t"
    "v_mfma_f32_16x16x32_bf16 v[88:91], v[116:119], v[164:167], v[88:91]\n\t"
    "v_xor_b32 v56, 0x80000000, v35\n\t"
    "v_xor_b32 v60, 0x80000000, v36\n\t"
    "v_mov_b32 v57, v56\n\t"
    "v_mfma_f32_16x16x32_bf16 v[92:95], v[116:119], v[180:183], v[92:95]\n\t"
    "ds_read_b64_tr_b16 v[116:117], v34 offset:41984\n\t"
    "ds_read_b64_tr_b16 v[118:119], v34 offset:46080\n\t"
    "v_mov_b32 v61, v60\n\t"
    "v_pk_add_f32 v[64:65], v[64:65], v[56:57]\n\t"
    "v_pk_add_f32 v[66:67], v[66:67], v[56:57]\n\t"
    "v_mfma_f32_16x16x32_bf16 v[88:91], v[120:123], v[168:171], v[88:91]\n\t"
    "v_pk_add_f32 v[68:69], v[68:69], v[60:61]\n\t"
    "v_pk_add_f32 v[70:71], v[70:71], v[60:61]\n\t"
    "v_pk_add_f32 v[72:73], v[72:73], v[56:57]\n\t"
    "v_mfma_f32_16x16x32_bf16 v[92:95], v[120:123], v[184:187], v[92:95]\n\t"
    "ds_read_b64_tr_b16 v[120:121], v33 offset:42496\n\t"
    "ds_read_b64_tr_b16 v[122:123], v33 offset:46592\n\t"
    "v_pk_add_f32 v[74:75], v[74:75], v[56:57]\n\t"
    "v_pk_add_f32 v[76:77], v[76:77], v[60:61]\n\t"
    "v_pk_add_f32 v[78:79], v[78:79], v[60:61]\n\t"
    "v_mfma_f32_16x16x32_bf16 v[88:91], v[124:127], v[172:175], v[88:91]\n\t"
    "v_exp_f32 v64, v64\n\t"
    "v_exp_f32 v65, v65\n\t"
    "v_exp_f32 v66, v66\n\t"
    "v_mfma_f32_16x16x32_bf16 v[92:95], v[124:127], v[188:191], v[92:95]\n\t"
    "ds_read_b64_tr_b16 v[124:125], v34 offset:42496\n\t"
    "ds_read_b64_tr_b16 v[126:127], v34 offset:46592\n\t"
    "v_exp_f32 v67, v67\n\t"
    "v_exp_f32 v68, v68\n\t"
    "v_exp_f32 v69, v69\n\t"
    "ds_read_b32 v37, v38\n\t"
    "ds_write_b32 v28, v39\n\t"
    "v_exp_f32 v70, v70\n\t"
    "v_exp_f32 v71, v71\n\t"
    "v_exp_f32 v72, v72\n\t"
    "v_exp_f32 v73, v73\n\t"
    "v_exp_f32 v74, v74\n\t"
    "v_exp_f32 v75, v75\n\t"
    "v_exp_f32 v76, v76\n\t"
    "v_exp_f32 v77, v77\n\t"
    "v_exp_f32 v78, v78\n\t"
    "v_exp_f32 v79, v79\n\t"
    "v_mov_b64 v[58:59], v[56:57]\n\t"
    "v_cvt_pk_bf16_f32 v40, v64, v65\n\t"
    "v_cvt_pk_bf16_f32 v41, v66, v67\n\t"
    "v_cvt_pk_bf16_f32 v44, v68, v69\n\t"
    "v_cvt_pk_bf16_f32 v45, v70, v71\n\t"
    "v_cvt_pk_bf16_f32 v42, v72, v73\n\t"
    "v_cvt_pk_bf16_f32 v43, v74, v75\n\t"
    "v_cvt_pk_bf16_f32 v46, v76, v77\n\t"
    "v_cvt_pk_bf16_f32 v47, v78, v79\n\t"
    "v_mov_b64 v[62:63], v[60:61]\n\t"
    "v_add_u32 v32, 49152, v29\n\t"
    "v_add_u32 v33, 49152, v30\n\t"
    "v_add_u32 v34, 49152, v31\n\t"
    "s_waitcnt lgkmcnt(0)\n\t"
    "v_readfirstlane_b32 s34, v37\n\t"
    "v_mfma_f32_16x16x32_bf16 v[192:195], v[96:99], v[40:43], v[192:195]\n\t"
    "v_pk_add_f32 v[80:81], v[80:81], v[56:57]\n\t"
    "v_pk_add_f32 v[82:83], v[82:83], v[56:57]\n\t"
    "v_mfma_f32_16x16x32_bf16 v[196:199], v[96:99], v[44:47], v[196:199]\n\t"
    "v_pk_add_f32 v[84:85], v[84:85], v[60:61]\n\t"
    "v_pk_add_f32 v[86:87], v[86:87], v[60:61]\n\t"
    "v_mfma_f32_16x16x32_bf16 v[200:203], v[100:103], v[40:43], v[200:203]\n\t"
    "v_pk_add_f32 v[88:89], v[88:89], v[56:57]\n\t"
    "v_pk_add_f32 v[90:91], v[90:91], v[56:57]\n\t"
    "v_mfma_f32_16x16x32_bf16 v[204:207], v[100:103], v[44:47], v[204:207]\n\t"
    "v_pk_add_f32 v[76:77], v[92:93], v[60:61]\n\t"
    "v_pk_add_f32 v[78:79], v[94:95], v[60:61]\n\t"
    "v_mfma_f32_16x16x32_bf16 v[208:211], v[104:107], v[40:43], v[208:211]\n\t"
    "v_exp_f32 v80, v80\n\t"
    "v_exp_f32 v81, v81\n\t"
    "v_mfma_f32_16x16x32_bf16 v[212:215], v[104:107], v[44:47], v[212:215]\n\t"
    "v_exp_f32 v82, v82\n\t"
    "v_exp_f32 v83, v83\n\t"
    "s_cmp_ge_i32 s34, 5\n\t"
    "s_cbranch_scc1 .Lqc%=\n\t"
    ".Lpc%=:\n\t"
    "ds_read_b32 v37, v38\n\t"
    "s_waitcnt lgkmcnt(0)\n\t"
    "v_readfirstlane_b32 s34, v37\n\t"
    "s_cmp_ge_i32 s34, 5\n\t"
    "s_cbranch_scc1 .Lqc%=\n\t"
    "s_sleep 1\n\t"
    "s_branch .Lpc%=\n\t"
    ".Lqc%=:\n\t"
    "v_mfma_f32_16x16x32_bf16 v[216:219], v[108:111], v[40:43], v[216:219]\n\t"
    "v_exp_f32 v84, v84\n\t"
    "v_exp_f32 v85, v85\n\t"
    "ds_read_b128 v[128:131], v32 offset:0\n\t"
    "ds_read_b64_tr_b16 v[96:97], v33 offset:8192\n\t"
    "ds_read_b64_tr_b16 v[98:99], v33 offset:12288\n\t"
    "v_mfma_f32_16x16x32_bf16 v[220:223], v[108:111], v[44:47], v[220:223]\n\t"
    "v_exp_f32 v86, v86\n\t"
    "v_exp_f32 v87, v87\n\t"
    "ds_read_b128 v[132:135], v32 offset:512\n\t"
    "ds_read_b64_tr_b16 v[100:101], v34 offset:8192\n\t"
    "ds_read_b64_tr_b16 v[102:103], v34 offset:12288\n\t"
    "v_mfma_f32_16x16x32_bf16 v[224:227], v[112:115], v[40:43], v[224:227]\n\t"
    "v_exp_f32 v72, v88\n\t"
    "v_exp_f32 v73, v89\n\t"
    "ds_read_b128 v[136:139], v32 offset:1024\n\t"
    "ds_read_b64_tr_b16 v[104:105], v33 offset:8704\n\t"
    "ds_read_b64_tr_b16 v[106:107], v33 offset:12800\n\t"
    "v_mfma_f32_16x16x32_bf16 v[228:231], v[112:115], v[44:47], v[228:231]\n\t"
    "v_exp_f32 v74, v90\n\t"
    "ds_read_b128 v[140:143], v32 offset:1536\n\t"
    "ds_read_b64_tr_b16 v[108:109], v34 offset:8704\n\t"
    "ds_read_b64_tr_b16 v[110:111], v34 offset:12800\n\t"
    "v_mfma_f32_16x16x32_bf16 v[232:235], v[116:119], v[40:43], v[232:235]\n\t"
    "v_exp_f32 v75, v91\n\t"
    "ds_read_b128 v[144:147], v32 offset:4096\n\t"
    "ds_read_b64_tr_b16 v[112:113], v33 offset:9216\n\t"
    "ds_read_b64_tr_b16 v[114:115], v33 offset:13312\n\t"
    "v_mfma_f32_16x16x32_bf16 v[236:239], v[116:119], v[44:47], v[236:239]\n\t"
    "v_cvt_pk_bf16_f32 v48, v80, v81\n\t"
    "ds_read_b128 v[148:151], v32 offset:4608\n\t"
    "ds_read_b64_tr_b16 v[116:117], v34 offset:9216\n\t"
    "ds_read_b64_tr_b16 v[118:119], v34 offset:13312\n\t"
    "v_mfma_f32_16x16x32_bf16 v[240:243], v[120:123], v[40:43], v[240:243]\n\t"
    "v_cvt_pk_bf16_f32 v49, v82, v83\n\t"
    "ds_read_b128 v[152:155], v32 offset:5120\n\t"
    "v_mfma_f32_16x16x32_bf16 v[244:247], v[120:123], v[44:47], v[244:247]\n\t"
    "v_cvt_pk_bf16_f32 v52, v84, v85\n\t"
    "ds_read_b128 v[156:159], v32 offset:5632\n\t"
    "ds_read_b64_tr_b16 v[120:121], v33 offset:9728\n\t"
    "ds_read_b64_tr_b16 v[122:123], v33 offset:13824\n\t"
    "ds_read_b32 v37, v38\n\t"
    "v_mfma_f32_16x16x32_bf16 v[248:251], v[124:127], v[40:43], v[248:251]\n\t"
    "v_cvt_pk_bf16_f32 v53, v86, v87\n\t"
    "v_mfma_f32_16x16x32_bf16 v[252:255], v[124:127], v[44:47], v[252:255]\n\t"
    "v_cvt_pk_bf16_f32 v50, v72, v73\n\t"
    "ds_read_b64_tr_b16 v[124:125], v34 offset:9728\n\t"
    "ds_read_b64_tr_b16 v[126:127], v34 offset:13824\n\t"
    "v_mov_b32 v80, 0x3f803f80\n\t"
    "v_mov_b32 v81, 0x3f803f80\n\t"
    "v_mov_b32 v82, 0x3f803f80\n\t"
    "v_mov_b32 v83, 0x3f803f80\n\t"
    "v_mov_b32 v39, 3\n\t"
    "s_mov_b32 s51, 7\n\t"
    "s_mov_b32 s33, 65536\n\t"
    "v_add_u32 v32, 65536, v29\n\t"
    "v_mfma_f32_16x16x32_bf16 v[84:87], v[80:83], v[40:43], 0\n\t"
    "v_mfma_f32_16x16x32_bf16 v[88:91], v[80:83], v[44:47], 0\n\t"
    "s_waitcnt vmcnt(0) lgkmcnt(2)\n\t"
    "v_readfirstlane_b32 s34, v37\n\t"
    "v_mov_b32 v24, 34304\n\t"
    "global_load_dword v10, v24, s[16:17] sc1\n\t"
    "s_branch .Lchk1%=\n\t"
    ".Lloop%=:\n\t"
    "v_mfma_f32_16x16x32_bf16 v[64:67], v[128:131], v[160:163], v[56:59]\n\t"
    "v_exp_f32 v76, v76\n\t"
    "v_mfma_f32_16x16x32_bf16 v[68:71], v[128:131], v[176:179], v[60:63]\n\t"
    "ds_read_b128 v[128:131], v32 offset:0\n\t"
    "v_cvt_pk_bf16_f32 v51, v74, v75\n\t"
    "v_mfma_f32_16x16x32_bf16 v[64:67], v[132:135], v[164:167], v[64:67]\n\t"
    "v_exp_f32 v77, v77\n\t"
    "v_mfma_f32_16x16x32_bf16 v[68:71], v[132:135], v[180:183], v[68:71]\n\t"
    "ds_read_b128 v[132:135], v32 offset:512\n\t"
    "ds_write_b32 v28, v39\n\t"
    "v_mfma_f32_16x16x32_bf16 v[64:67], v[136:139], v[168:171], v[64:67]\n\t"
    "v_exp_f32 v78, v78\n\t"
    "v_mfma_f32_16x16x32_bf16 v[68:71], v[136:139], v[184:187], v[68:71]\n\t"
    "ds_read_b128 v[136:139], v32 offset:1024\n\t"
    "v_cvt_pk_bf16_f32 v54, v76, v77\n\t"
    "v_mfma_f32_16x16x32_bf16 v[64:67], v[140:143], v[172:175], v[64:67]\n\t"
    "v_exp_f32 v79, v79\n\t"
    "v_mfma_f32_16x16x32_bf16 v[68:71], v[140:143], v[188:191], v[68:71]\n\t"
    "ds_read_b128 v[140:143], v32 offset:1536\n\t"
    "v_add_u32 v33, s33, v30\n\t"
    "v_mfma_f32_16x16x32_bf16 v[72:75], v[144:147], v[160:163], v[56:59]\n\t"
    "v_cvt_pk_bf16_f32 v55, v78, v79\n\t"
    "v_add_u32 v34, s33, v31\n\t"
    "v_mfma_f32_16x16x32_bf16 v[76:79], v[144:147], v[176:179], v[60:63]\n\t"
    "ds_read_b128 v[144:147], v32 offset:4096\n\t"
    "v_add_u32 v39, 2, v39\n\t"
    "v_mfma_f32_16x16x32_bf16 v[72:75], v[148:151], v[164:167], v[72:75]\n\t"
    "ds_read_b32 v37, v38\n\t"
    "s_add_i32 s51, s51, 2\n\t"
    "v_mfma_f32_16x16x32_bf16 v[76:79], v[148:151], v[180:183], v[76:79]\n\t"
    "v_exp_f32 v64, v64\n\t"
    "ds_read_b128 v[148:151], v32 offset:4608\n\t"
    "v_mfma_f32_16x16x32_bf16 v[72:75], v[152:155], v[168:171], v[72:75]\n\t"
    "v_exp_f32 v65, v65\n\t"
    "v_mfma_f32_16x16x32_bf16 v[76:79], v[152:155], v[184:187], v[76:79]\n\t"
    "v_exp_f32 v66, v66\n\t"
    "ds_read_b128 v[152:155], v32 offset:5120\n\t"
    "v_mfma_f32_16x16x32_bf16 v[72:75], v[156:159], v[172:175], v[72:75]\n\t"
    "v_exp_f32 v67, v67\n\t"
    "v_mfma_f32_16x16x32_bf16 v[76:79], v[156:159], v[188:191], v[76:79]\n\t"
    "ds_read_b128 v[156:159], v32 offset:5632\n\t"
    "s_waitcnt lgkmcnt(9)\n\t"
    "v_mfma_f32_16x16x32_bf16 v[192:195], v[96:99], v[48:51], v[192:195]\n\t"
    "v_exp_f32 v68, v68\n\t"
    "v_mfma_f32_16x16x32_bf16 v[196:199], v[96:99], v[52:55], v[196:199]\n\t"
    "ds_read_b64_tr_b16 v[96:97], v33 offset:8192\n\t"
    "ds_read_b64_tr_b16 v[98:99], v33 offset:12288\n\t"
    "v_cvt_pk_bf16_f32 v40, v64, v65\n\t"
    "v_mfma_f32_16x16x32_bf16 v[200:203], v[100:103], v[48:51], v[200:203]\n\t"
    "v_exp_f32 v69, v69\n\t"
    "v_mfma_f32_16x16x32_bf16 v[204:207], v[100:103], v[52:55], v[204:207]\n\t"
    "ds_read_b64_tr_b16 v[100:101], v34 offset:8192\n\t"
    "ds_read_b64_tr_b16 v[102:103], v34 offset:12288\n\t"
    "v_mfma_f32_16x16x32_bf16 v[84:87], v[80:83], v[48:51], v[84:87]\n\t"
    "v_exp_f32 v70, v70\n\t"
    "v_mfma_f32_16x16x32_bf16 v[208:211], v[104:107], v[48:51], v[208:211]\n\t"
    "v_exp_f32 v71, v71\n\t"
    "v_mfma_f32_16x16x32_bf16 v[212:215], v[104:107], v[52:55], v[212:215]\n\t"
    "ds_read_b64_tr_b16 v[104:105], v33 offset:8704\n\t"
    "ds_read_b64_tr_b16 v[106:107], v33 offset:12800\n\t"
    "v_cvt_pk_bf16_f32 v41, v66, v67\n\t"
    "v_mfma_f32_16x16x32_bf16 v[216:219], v[108:111], v[48:51], v[216:219]\n\t"
    "v_exp_f32 v72, v72\n\t"
    "v_mfma_f32_16x16x32_bf16 v[220:223], v[108:111], v[52:55], v[220:223]\n\t"
    "ds_read_b64_tr_b16 v[108:109], v34 offset:8704\n\t"
    "ds_read_b64_tr_b16 v[110:111], v34 offset:12800\n\t"
    "v_mfma_f32_16x16x32_bf16 v[224:227], v[112:115], v[48:51], v[224:227]\n\t"
    "v_exp_f32 v73, v73\n\t"
    "v_mfma_f32_16x16x32_bf16 v[228:231], v[112:115], v[52:55], v[228:231]\n\t"
    "ds_read_b64_tr_b16 v[112:113], v33 offset:9216\n\t"
    "ds_read_b64_tr_b16 v[114:115], v33 offset:13312\n\t"
    "v_mfma_f32_16x16x32_bf16 v[232:235], v[116:119], v[48:51], v[232:235]\n\t"
    "v_cvt_pk_bf16_f32 v44, v68, v69\n\t"
    "v_cvt_pk_bf16_f32 v45, v70, v71\n\t"
    "v_mfma_f32_16x16x32_bf16 v[236:239], v[116:119], v[52:55], v[236:239]\n\t"
    "ds_read_b64_tr_b16 v[116:117], v34 offset:9216\n\t"
    "ds_read_b64_tr_b16 v[118:119], v34 offset:13312\n\t"
    "v_mfma_f32_16x16x32_bf16 v[88:91], v[80:83], v[52:55], v[88:91]\n\t"
    "v_exp_f32 v74, v74\n\t"
    "v_mfma_f32_16x16x32_bf16 v[240:243], v[120:123], v[48:51], v[240:243]\n\t"
    "v_exp_f32 v75, v75\n\t"
    "v_mfma_f32_16x16x32_bf16 v[244:247], v[120:123], v[52:55], v[244:247]\n\t"
    "ds_read_b64_tr_b16 v[120:121], v33 offset:9728\n\t"
    "ds_read_b64_tr_b16 v[122:123], v33 offset:13824\n\t"
    "s_waitcnt lgkmcnt(14)\n\t"
    "v_mfma_f32_16x16x32_bf16 v[248:251], v[124:127], v[48:51], v[248:251]\n\t"
    "v_cvt_pk_bf16_f32 v42, v72, v73\n\t"
    "v_readfirstlane_b32 s34, v37\n\t"
    "v_mfma_f32_16x16x32_bf16 v[252:255], v[124:127], v[52:55], v[252:255]\n\t"
    "ds_read_b64_tr_b16 v[124:125], v34 offset:9728\n\t"
    "ds_read_b64_tr_b16 v[126:127], v34 offset:13824\n\t"
    "s_cmp_lt_i32 s34, s51\n\t"
    "s_cbranch_scc1 .Lpoll0%=\n\t"
    ".Lcont0%=:\n\t"
    "v_mfma_f32_16x16x32_bf16 v[64:67], v[128:131], v[160:163], v[56:59]\n\t"
    "v_exp_f32 v76, v76\n\t"
    "v_mfma_f32_16x16x32_bf16 v[68:71], v[128:131], v[176:179], v[60:63]\n\t"
    "ds_read_b128 v[128:131], v32 offset:16384\n\t"
    "v_cvt_pk_bf16_f32 v43, v74, v75\n\t"
    "v_mfma_f32_16x16x32_bf16 v[64:67], v[132:135], v[164:167], v[64:67]\n\t"
    "v_exp_f32 v77, v77\n\t"
    "v_mfma_f32_16x16x32_bf16 v[68:71], v[132:135], v[180:183], v[68:71]\n\t"
    "ds_read_b128 v[132:135], v32 offset:16896\n\t"
    "s_add_u32 s33, s33, 0x8000\n\t"
    "v_mfma_f32_16x16x32_bf16 v[64:67], v[136:139], v[168:171], v[64:67]\n\t"
    "v_exp_f32 v78, v78\n\t"
    "v_mfma_f32_16x16x32_bf16 v[68:71], v[136:139], v[184:187], v[68:71]\n\t"
    "ds_read_b128 v[136:139], v32 offset:17408\n\t"
    "v_cvt_pk_bf16_f32 v46, v76, v77\n\t"
    "v_mfma_f32_16x16x32_bf16 v[64:67], v[140:143], v[172:175], v[64:67]\n\t"
    "v_exp_f32 v79, v79\n\t"
    "v_mfma_f32_16x16x32_bf16 v[68:71], v[140:143], v[188:191], v[68:71]\n\t"
    "ds_read_b128 v[140:143], v32 offset:17920\n\t"
    "s_and_b32 s33, s33, 0x1ffff\n\t"
    "v_mfma_f32_16x16x32_bf16 v[72:75], v[144:147], v[160:163], v[56:59]\n\t"
    "v_cvt_pk_bf16_f32 v47, v78, v79\n\t"
    "s_add_i32 s51, s51, 2\n\t"
    "v_mfma_f32_16x16x32_bf16 v[76:79], v[144:147], v[176:179], v[60:63]\n\t"
    "ds_read_b128 v[144:147], v32 offset:20480\n\t"
    "v_mfma_f32_16x16x32_bf16 v[72:75], v[148:151], v[164:167], v[72:75]\n\t"
    "ds_read_b32 v37, v38\n\t"
    "v_mfma_f32_16x16x32_bf16 v[76:79], v[148:151], v[180:183], v[76:79]\n\t"
    "v_exp_f32 v64, v64\n\t"
    "ds_read_b128 v[148:151], v32 offset:20992\n\t"
    "v_mfma_f32_16x16x32_bf16 v[72:75], v[152:155], v[168:171], v[72:75]\n\t"
    "v_exp_f32 v65, v65\n\t"
    "v_mfma_f32_16x16x32_bf16 v[76:79], v[152:155], v[184:187], v[76:79]\n\t"
    "v_exp_f32 v66, v66\n\t"
    "ds_read_b128 v[152:155], v32 offset:21504\n\t"
    "v_mfma_f32_16x16x32_bf16 v[72:75], v[156:159], v[172:175], v[72:75]\n\t"
    "v_exp_f32 v67, v67\n\t"
    "v_mfma_f32_16x16x32_bf16 v[76:79], v[156:159], v[188:191], v[76:79]\n\t"
    "ds_read_b128 v[156:159], v32 offset:22016\n\t"
    "s_waitcnt lgkmcnt(9)\n\t"
    "v_mfma_f32_16x16x32_bf16 v[192:195], v[96:99], v[40:43], v[192:195]\n\t"
    "v_exp_f32 v68, v68\n\t"
    "v_mfma_f32_16x16x32_bf16 v[196:199], v[96:99], v[44:47], v[196:199]\n\t"
    "ds_read_b64_tr_b16 v[96:97], v33 offset:24576\n\t"
    "ds_read_b64_tr_b16 v[98:99], v33 offset:28672\n\t"
    "v_cvt_pk_bf16_f32 v48, v64, v65\n\t"
    "v_mfma_f32_16x16x32_bf16 v[200:203], v[100:103], v[40:43], v[200:203]\n\t"
    "v_exp_f32 v69, v69\n\t"
    "v_mfma_f32_16x16x32_bf16 v[204:207], v[100:103], v[44:47], v[204:207]\n\t"
    "ds_read_b64_tr_b16 v[100:101], v34 offset:24576\n\t"
    "ds_read_b64_tr_b16 v[102:103], v34 offset:28672\n\t"
    "v_add_u32 v32, s33, v29\n\t"
    "v_mfma_f32_16x16x32_bf16 v[84:87], v[80:83], v[40:43], v[84:87]\n\t"
    "v_exp_f32 v70, v70\n\t"
    "v_mfma_f32_16x16x32_bf16 v[208:211], v[104:107], v[40:43], v[208:211]\n\t"
    "v_exp_f32 v71, v71\n\t"
    "v_mfma_f32_16x16x32_bf16 v[212:215], v[104:107], v[44:47], v[212:215]\n\t"
    "ds_read_b64_tr_b16 v[104:105], v33 offset:25088\n\t"
    "ds_read_b64_tr_b16 v[106:107], v33 offset:29184\n\t"
    "v_cvt_pk_bf16_f32 v49, v66, v67\n\t"
    "v_mfma_f32_16x16x32_bf16 v[216:219], v[108:111], v[40:43], v[216:219]\n\t"
    "v_exp_f32 v72, v72\n\t"
    "v_mfma_f32_16x16x32_bf16 v[220:223], v[108:111], v[44:47], v[220:223]\n\t"
    "ds_read_b64_tr_b16 v[108:109], v34 offset:25088\n\t"
    "ds_read_b64_tr_b16 v[110:111], v34 offset:29184\n\t"
    "v_mfma_f32_16x16x32_bf16 v[224:227], v[112:115], v[40:43], v[224:227]\n\t"
    "v_exp_f32 v73, v73\n\t"
    "v_mfma_f32_16x16x32_bf16 v[228:231], v[112:115], v[44:47], v[228:231]\n\t"
    "ds_read_b64_tr_b16 v[112:113], v33 offset:25600\n\t"
    "ds_read_b64_tr_b16 v[114:115], v33 offset:29696\n\t"
    "v_mfma_f32_16x16x32_bf16 v[232:235], v[116:119], v[40:43], v[232:235]\n\t"
    "v_cvt_pk_bf16_f32 v52, v68, v69\n\t"
    "v_cvt_pk_bf16_f32 v53, v70, v71\n\t"
    "v_mfma_f32_16x16x32_bf16 v[236:239], v[116:119], v[44:47], v[236:239]\n\t"
    "ds_read_b64_tr_b16 v[116:117], v34 offset:25600\n\t"
    "ds_read_b64_tr_b16 v[118:119], v34 offset:29696\n\t"
    "v_mfma_f32_16x16x32_bf16 v[88:91], v[80:83], v[44:47], v[88:91]\n\t"
    "v_exp_f32 v74, v74\n\t"
    "v_mfma_f32_16x16x32_bf16 v[240:243], v[120:123], v[40:43], v[240:243]\n\t"
    "v_exp_f32 v75, v75\n\t"
    "v_mfma_f32_16x16x32_bf16 v[244:247], v[120:123], v[44:47], v[244:247]\n\t"
    "ds_read_b64_tr_b16 v[120:121], v33 offset:26112\n\t"
    "ds_read_b64_tr_b16 v[122:123], v33 offset:30208\n\t"
    "s_waitcnt lgkmcnt(14)\n\t"
    "v_mfma_f32_16x16x32_bf16 v[248:251], v[124:127], v[40:43], v[248:251]\n\t"
    "v_cvt_pk_bf16_f32 v50, v72, v73\n\t"
    "v_readfirstlane_b32 s34, v37\n\t"
    "v_mfma_f32_16x16x32_bf16 v[252:255], v[124:127], v[44:47], v[252:255]\n\t"
    "ds_read_b64_tr_b16 v[124:125], v34 offset:26112\n\t"
    "ds_read_b64_tr_b16 v[126:127], v34 offset:30208\n\t"
    ".Lchk1%=:\n\t"
    "s_cmp_ge_i32 s34, s51\n\t"
    "s_cbranch_scc1 .Lloop%=\n\t"
    "s_cmp_eq_i32 s51, 35\n\t"
    "s_cbranch_scc1 .Ltail%=\n\t"
    ".Lpoll1%=:\n\t"
    "ds_read_b32 v37, v38\n\t"
    "s_waitcnt lgkmcnt(0)\n\t"
    "v_readfirstlane_b32 s34, v37\n\t"
    "s_cmp_ge_i32 s34, s51\n\t"
    "s_cbranch_scc1 .Lloop%=\n\t"
    "s_sleep 1\n\t"
    "s_branch .Lpoll1%=\n\t"
    ".Lpoll0%=:\n\t"
    "ds_read_b32 v37, v38\n\t"
    "s_waitcnt lgkmcnt(0)\n\t"
    "v_readfirstlane_b32 s34, v37\n\t"
    "s_cmp_ge_i32 s34, s51\n\t"
    "s_cbranch_scc1 .Lcont0%=\n\t"
    "s_sleep 1\n\t"
    "s_branch .Lpoll0%=\n\t"
    ".Ltail%=:\n\t"
    "v_cvt_pk_bf16_f32 v51, v74, v75\n\t"
    "v_exp_f32 v76, v76\n\t"
    "v_mov_b32 v39, 17\n\t"
    "s_waitcnt lgkmcnt(8)\n\t"
    "v_mfma_f32_16x16x32_bf16 v[84:87], v[80:83], v[48:51], v[84:87]\n\t"
    "v_exp_f32 v77, v77\n\t"
    "v_mfma_f32_16x16x32_bf16 v[192:195], v[96:99], v[48:51], v[192:195]\n\t"
    "v_exp_f32 v78, v78\n\t"
    "v_mfma_f32_16x16x32_bf16 v[200:203], v[100:103], v[48:51], v[200:203]\n\t"
    "v_exp_f32 v79, v79\n\t"
    "v_cvt_pk_bf16_f32 v54, v76, v77\n\t"
    "v_mfma_f32_16x16x32_bf16 v[208:211], v[104:107], v[48:51], v[208:211]\n\t"
    "v_cvt_pk_bf16_f32 v55, v78, v79\n\t"
    "ds_write_b32 v28, v39\n\t"
    "v_mfma_f32_16x16x32_bf16 v[216:219], v[108:111], v[48:51], v[216:219]\n\t"
    "v_mfma_f32_16x16x32_bf16 v[196:199], v[96:99], v[52:55], v[196:199]\n\t"
    "v_mfma_f32_16x16x32_bf16 v[204:207], v[100:103], v[52:55], v[204:207]\n\t"
    "v_mfma_f32_16x16x32_bf16 v[88:91], v[80:83], v[52:55], v[88:91]\n\t"
    "v_mfma_f32_16x16x32_bf16 v[212:215], v[104:107], v[52:55], v[212:215]\n\t"
    "v_mfma_f32_16x16x32_bf16 v[220:223], v[108:111], v[52:55], v[220:223]\n\t"
    "s_waitcnt lgkmcnt(0)\n\t"
    "v_mfma_f32_16x16x32_bf16 v[224:227], v[112:115], v[48:51], v[224:227]\n\t"
    "v_mfma_f32_16x16x32_bf16 v[228:231], v[112:115], v[52:55], v[228:231]\n\t"
    "v_mfma_f32_16x16x32_bf16 v[232:235], v[116:119], v[48:51], v[232:235]\n\t"
    "v_mfma_f32_16x16x32_bf16 v[236:239], v[116:119], v[52:55], v[236:239]\n\t"
    "v_cvt_pk_bf16_f32 v64, v192, v193\n\t"
    "v_cvt_pk_bf16_f32 v65, v194, v195\n\t"
    "v_mfma_f32_16x16x32_bf16 v[240:243], v[120:123], v[48:51], v[240:243]\n\t"
    "v_cvt_pk_bf16_f32 v66, v196, v197\n\t"
    "v_cvt_pk_bf16_f32 v67, v198, v199\n\t"
    "v_mfma_f32_16x16x32_bf16 v[244:247], v[120:123], v[52:55], v[244:247]\n\t"
    "v_cvt_pk_bf16_f32 v68, v200, v201\n\t"
    "v_cvt_pk_bf16_f32 v69, v202, v203\n\t"
    "v_mfma_f32_16x16x32_bf16 v[248:251], v[124:127], v[48:51], v[248:251]\n\t"
    "v_cvt_pk_bf16_f32 v70, v204, v205\n\t"
    "v_cvt_pk_bf16_f32 v71, v206, v207\n\t"
    "v_mfma_f32_16x16x32_bf16 v[252:255], v[124:127], v[52:55], v[252:255]\n\t"
    "global_store_dwordx4 v12, v[64:67], s[16:17] sc1\n\t"
    "global_store_dwordx4 v12, v[68:71], s[16:17] offset:1024 sc1\n\t"
    "s_branch .Lpub1%=\n\t"
    ".Lpub%=:\n\t"
    "v_cvt_pk_bf16_f32 v64, v192, v193\n\t"
    "v_cvt_pk_bf16_f32 v65, v194, v195\n\t"
    "v_cvt_pk_bf16_f32 v66, v196, v197\n\t"
    "v_cvt_pk_bf16_f32 v67, v198, v199\n\t"
    "v_cvt_pk_bf16_f32 v68, v200, v201\n\t"
    "v_cvt_pk_bf16_f32 v69, v202, v203\n\t"
    "v_cvt_pk_bf16_f32 v70, v204, v205\n\t"
    "v_cvt_pk_bf16_f32 v71, v206, v207\n\t"
    "global_store_dwordx4 v12, v[64:67], s[16:17] sc1\n\t"
    "global_store_dwordx4 v12, v[68:71], s[16:17] offset:1024 sc1\n\t"
    ".Lpub1%=:\n\t"
    "v_cvt_pk_bf16_f32 v72, v208, v209\n\t"
    "v_cvt_pk_bf16_f32 v73, v210, v211\n\t"
    "v_cvt_pk_bf16_f32 v74, v212, v213\n\t"
    "v_cvt_pk_bf16_f32 v75, v214, v215\n\t"
    "v_cvt_pk_bf16_f32 v76, v216, v217\n\t"
    "v_cvt_pk_bf16_f32 v77, v218, v219\n\t"
    "v_cvt_pk_bf16_f32 v78, v220, v221\n\t"
    "v_cvt_pk_bf16_f32 v79, v222, v223\n\t"
    "global_store_dwordx4 v13, v[72:75], s[16:17] sc1\n\t"
    "global_store_dwordx4 v13, v[76:79], s[16:17] offset:1024 sc1\n\t"
    "v_cvt_pk_bf16_f32 v128, v224, v225\n\t"
    "v_cvt_pk_bf16_f32 v129, v226, v227\n\t"
    "v_cvt_pk_bf16_f32 v130, v228, v229\n\t"
    "v_cvt_pk_bf16_f32 v131, v230, v231\n\t"
    "v_cvt_pk_bf16_f32 v132, v232, v233\n\t"
    "v_cvt_pk_bf16_f32 v133, v234, v235\n\t"
    "v_cvt_pk_bf16_f32 v134, v236, v237\n\t"
    "v_cvt_pk_bf16_f32 v135, v238, v239\n\t"
    "global_store_dwordx4 v14, v[128:131], s[16:17] sc1\n\t"
    "global_store_dwordx4 v14, v[132:135], s[16:17] offset:1024 sc1\n\t"
    "v_and_b32 v24, 16, v1\n\t"
    "v_cmp_gt_u32 s[48:49], 32, v1\n\t"
    "v_cmp_eq_u32 s[58:59], 0, v24\n\t"
    "s_mov_b32 s36, 0x53800000\n\t"
    "v_add_f32 v25, v84, v88\n\t"
    "v_cmp_nle_f32 vcc, v25, s36\n\t"
    "s_nop 0\n\t"
    "s_cbranch_vccnz .Lslow%=\n\t"
    ".Lpub2%=:\n\t"
    "v_cndmask_b32 v24, v36, v35, s[58:59]\n\t"
    "v_cndmask_b32 v25, v88, v84, s[58:59]\n\t"
    "v_cndmask_b32 v24, v25, v24, s[48:49]\n\t"
    "global_store_dword v18, v24, s[16:17] sc1\n\t"
    "s_waitcnt vmcnt(7)\n\t"
    "v_add_u32 v92, 1, v10\n\t"
    "v_xor_b32 v93, 0x9e3779b9, v92\n\t"
    "s_mov_b64 exec, 8\n\t"
    "s_waitcnt vmcnt(0)\n\t"
    "global_atomic_swap_x2 v26, v[92:93], s[16:17] sc1\n\t"
    "s_mov_b32 s36, 0x100000\n\t"
    "s_mov_b64 exec, 7\n\t"
    "v_mov_b64 v[146:147], 0\n\t"
    ".Lfp%=:\n\t"
    "global_atomic_add_x2 v[94:95], v26, v[146:147], s[16:17] sc0 sc1\n\t"
    "s_waitcnt vmcnt(0)\n\t"
    "v_cmp_ne_u64 vcc, v[94:95], v[92:93]\n\t"
    "s_sub_u32 s36, s36, 1\n\t"
    "s_cbranch_scc1 .Lfd%=\n\t"
    "s_and_b64 exec, exec, vcc\n\t"
    "s_cbranch_vccnz .Lfp%=\n\t"
    ".Lfd%=:\n\t"
    "s_mov_b64 exec, -1\n\t"
    "global_load_dwordx4 v[128:131], v19, s[16:17] sc1\n\t"
    "global_load_dwordx4 v[132:135], v20, s[16:17] sc1\n\t"
    "global_load_dwordx4 v[136:139], v21, s[16:17] sc1\n\t"
    "global_load_dwordx4 v[96:99], v15, s[16:17] sc1\n\t"
    "global_load_dwordx4 v[100:103], v15, s[16:17] offset:1024 sc1\n\t"
    "global_load_dwordx4 v[104:107], v16, s[16:17] sc1\n\t"
    "global_load_dwordx4 v[108:111], v16, s[16:17] offset:1024 sc1\n\t"
    "global_load_dwordx4 v[112:115], v17, s[16:17] sc1\n\t"
    "global_load_dwordx4 v[116:119], v17, s[16:17] offset:1024 sc1\n\t"
    "s_waitcnt vmcnt(6)\n\t"
    "v_max3_f32 v140, v35, v128, v132\n\t"
    "v_max3_f32 v141, v36, v129, v133\n\t"
    "v_max_f32 v140, v140, v136\n\t"
    "v_max_f32 v141, v141, v137\n\t"
    "v_sub_f32 v142, v35, v140\n\t"
    "v_sub_f32 v143, v36, v141\n\t"
    "v_sub_f32 v128, v128, v140\n\t"
    "v_sub_f32 v129, v129, v141\n\t"
    "v_sub_f32 v132, v132, v140\n\t"
    "v_sub_f32 v133, v133, v141\n\t"
    "v_sub_f32 v136, v136, v140\n\t"
    "v_sub_f32 v137, v137, v141\n\t"
    "v_exp_f32 v142, v142\n\t"
    "v_exp_f32 v143, v143\n\t"
    "v_exp_f32 v128, v128\n\t"
    "v_exp_f32 v129, v129\n\t"
    "v_exp_f32 v132, v132\n\t"
    "v_exp_f32 v133, v133\n\t"
    "v_exp_f32 v136, v136\n\t"
    "v_exp_f32 v137, v137\n\t"
    "v_mul_f32 v144, v142, v84\n\t"
    "v_mul_f32 v145, v143, v88\n\t"
    "v_fmac_f32 v144, v128, v130\n\t"
    "v_fmac_f32 v145, v129, v131\n\t"
    "v_fmac_f32 v144, v132, v134\n\t"
    "v_fmac_f32 v145, v133, v135\n\t"
    "v_fmac_f32 v144, v136, v138\n\t"
    "v_fmac_f32 v145, v137, v139\n\t"
    "v_rcp_f32 v144, v144\n\t"
    "v_rcp_f32 v145, v145\n\t"
    "s_nop 0\n\t"
    "v_mul_f32 v142, v142, v144\n\t"
    "v_mul_f32 v143, v143, v145\n\t"
    "v_mul_f32 v128, v128, v144\n\t"
    "v_mul_f32 v129, v129, v145\n\t"
    "v_mul_f32 v132, v132, v144\n\t"
    "v_mul_f32 v133, v133, v145\n\t"
    "v_mul_f32 v136, v136, v144\n\t"
    "v_mul_f32 v137, v137, v145\n\t"
    "v_mul_f32 v240, v240, v142\n\t"
    "v_mul_f32 v241, v241, v142\n\t"
    "v_mul_f32 v242, v242, v142\n\t"
    "v_mul_f32 v243, v243, v142\n\t"
    "v_mul_f32 v244, v244, v143\n\t"
    "v_mul_f32 v245, v245, v143\n\t"
    "v_mul_f32 v246, v246, v143\n\t"
    "v_mul_f32 v247, v247, v143\n\t"
    "v_mul_f32 v248, v248, v142\n\t"
    "v_mul_f32 v249, v249, v142\n\t"
    "v_mul_f32 v250, v250, v142\n\t"
    "v_mul_f32 v251, v251, v142\n\t"
    "v_mul_f32 v252, v252, v143\n\t"
    "v_mul_f32 v253, v253, v143\n\t"
    "v_mul_f32 v254, v254, v143\n\t"
    "v_mul_f32 v255, v255, v143\n\t"
    "s_waitcnt vmcnt(4)\n\t"
    "v_lshlrev_b32 v24, 16, v96\n\t"
    "v_and_b32 v25, s45, v96\n\t"
    "v_fmac_f32 v240, v128, v24\n\t"
    "v_fmac_f32 v241, v128, v25\n\t"
    "v_lshlrev_b32 v26, 16, v97\n\t"
    "v_and_b32 v27, s45, v97\n\t"
    "v_fmac_f32 v242, v128, v26\n\t"
    "v_fmac_f32 v243, v128, v27\n\t"
    "v_lshlrev_b32 v24, 16, v98\n\t"
    "v_and_b32 v25, s45, v98\n\t"
    "v_fmac_f32 v244, v129, v24\n\t"
    "v_fmac_f32 v245, v129, v25\n\t"
    "v_lshlrev_b32 v26, 16, v99\n\t"
    "v_and_b32 v27, s45, v99\n\t"
    "v_fmac_f32 v246, v129, v26\n\t"
    "v_fmac_f32 v247, v129, v27\n\t"
    "v_lshlrev_b32 v24, 16, v100\n\t"
    "v_and_b32 v25, s45, v100\n\t"
    "v_fmac_f32 v248, v128, v24\n\t"
    "v_fmac_f32 v249, v128, v25\n\t"
    "v_lshlrev_b32 v26, 16, v101\n\t"
    "v_and_b32 v27, s45, v101\n\t"
    "v_fmac_f32 v250, v128, v26\n\t"
    "v_fmac_f32 v251, v128, v27\n\t"
    "v_lshlrev_b32 v24, 16, v102\n\t"
    "v_and_b32 v25, s45, v102\n\t"
    "v_fmac_f32 v252, v129, v24\n\t"
    "v_fmac_f32 v253, v129, v25\n\t"
    "v_lshlrev_b32 v26, 16, v103\n\t"
    "v_and_b32 v27, s45, v103\n\t"
    "v_fmac_f32 v254, v129, v26\n\t"
    "v_fmac_f32 v255, v129, v27\n\t"
    "s_waitcnt vmcnt(2)\n\t"
    "v_lshlrev_b32 v24, 16, v104\n\t"
    "v_and_b32 v25, s45, v104\n\t"
    "v_fmac_f32 v240, v132, v24\n\t"
    "v_fmac_f32 v241, v132, v25\n\t"
    "v_lshlrev_b32 v26, 16, v105\n\t"
    "v_and_b32 v27, s45, v105\n\t"
    "v_fmac_f32 v242, v132, v26\n\t"
    "v_fmac_f32 v243, v132, v27\n\t"
    "v_lshlrev_b32 v24, 16, v106\n\t"
    "v_and_b32 v25, s45, v106\n\t"
    "v_fmac_f32 v244, v133, v24\n\t"
    "v_fmac_f32 v245, v133, v25\n\t"
    "v_lshlrev_b32 v26, 16, v107\n\t"
    "v_and_b32 v27, s45, v107\n\t"
    "v_fmac_f32 v246, v133, v26\n\t"
    "v_fmac_f32 v247, v133, v27\n\t"
    "v_lshlrev_b32 v24, 16, v108\n\t"
    "v_and_b32 v25, s45, v108\n\t"
    "v_fmac_f32 v248, v132, v24\n\t"
    "v_fmac_f32 v249, v132, v25\n\t"
    "v_lshlrev_b32 v26, 16, v109\n\t"
    "v_and_b32 v27, s45, v109\n\t"
    "v_fmac_f32 v250, v132, v26\n\t"
    "v_fmac_f32 v251, v132, v27\n\t"
    "v_lshlrev_b32 v24, 16, v110\n\t"
    "v_and_b32 v25, s45, v110\n\t"
    "v_fmac_f32 v252, v133, v24\n\t"
    "v_fmac_f32 v253, v133, v25\n\t"
    "v_lshlrev_b32 v26, 16, v111\n\t"
    "v_and_b32 v27, s45, v111\n\t"
    "v_fmac_f32 v254, v133, v26\n\t"
    "v_fmac_f32 v255, v133, v27\n\t"
    "s_waitcnt vmcnt(0)\n\t"
    "v_lshlrev_b32 v24, 16, v112\n\t"
    "v_and_b32 v25, s45, v112\n\t"
    "v_fmac_f32 v240, v136, v24\n\t"
    "v_fmac_f32 v241, v136, v25\n\t"
    "v_lshlrev_b32 v26, 16, v113\n\t"
    "v_and_b32 v27, s45, v113\n\t"
    "v_fmac_f32 v242, v136, v26\n\t"
    "v_fmac_f32 v243, v136, v27\n\t"
    "v_lshlrev_b32 v24, 16, v114\n\t"
    "v_and_b32 v25, s45, v114\n\t"
    "v_fmac_f32 v244, v137, v24\n\t"
    "v_fmac_f32 v245, v137, v25\n\t"
    "v_lshlrev_b32 v26, 16, v115\n\t"
    "v_and_b32 v27, s45, v115\n\t"
    "v_fmac_f32 v246, v137, v26\n\t"
    "v_fmac_f32 v247, v137, v27\n\t"
    "v_lshlrev_b32 v24, 16, v116\n\t"
    "v_and_b32 v25, s45, v116\n\t"
    "v_fmac_f32 v248, v136, v24\n\t"
    "v_fmac_f32 v249, v136, v25\n\t"
    "v_lshlrev_b32 v26, 16, v117\n\t"
    "v_and_b32 v27, s45, v117\n\t"
    "v_fmac_f32 v250, v136, v26\n\t"
    "v_fmac_f32 v251, v136, v27\n\t"
    "v_lshlrev_b32 v24, 16, v118\n\t"
    "v_and_b32 v25, s45, v118\n\t"
    "v_fmac_f32 v252, v137, v24\n\t"
    "v_fmac_f32 v253, v137, v25\n\t"
    "v_lshlrev_b32 v26, 16, v119\n\t"
    "v_and_b32 v27, s45, v119\n\t"
    "v_fmac_f32 v254, v137, v26\n\t"
    "v_fmac_f32 v255, v137, v27\n\t"
    "v_cvt_pk_bf16_f32 v64, v240, v241\n\t"
    "v_cvt_pk_bf16_f32 v65, v242, v243\n\t"
    "v_cvt_pk_bf16_f32 v66, v248, v249\n\t"
    "v_cvt_pk_bf16_f32 v67, v250, v251\n\t"
    "s_nop 1\n\t"
    "v_permlane16_swap_b32 v64, v66\n\t"
    "v_permlane16_swap_b32 v65, v67\n\t"
    "s_nop 1\n\t"
    "global_store_dwordx4 v22, v[64:67], s[14:15]\n\t"
    "v_cvt_pk_bf16_f32 v68, v244, v245\n\t"
    "v_cvt_pk_bf16_f32 v69, v246, v247\n\t"
    "v_cvt_pk_bf16_f32 v70, v252, v253\n\t"
    "v_cvt_pk_bf16_f32 v71, v254, v255\n\t"
    "s_nop 1\n\t"
    "v_permlane16_swap_b32 v68, v70\n\t"
    "v_permlane16_swap_b32 v69, v71\n\t"
    "v_add_u32 v22, 0x1000, v22\n\t"
    "s_nop 0\n\t"
    "global_store_dwordx4 v22, v[68:71], s[14:15]\n\t"
    "s_cmp_lg_u32 s22, 0\n\t"
    "s_cbranch_scc1 .Lnb%=\n\t"
    "v_mov_b32 v24, 34304\n\t"
    "s_mov_b64 exec, 1\n\t"
    "global_store_dword v24, v92, s[16:17] sc1\n\t"
    ".Lnb%=:\n\t"
    "s_endpgm\n\t"
    ".Lslow%=:\n\t"
    "s_cmp_eq_u32 s51, 0\n\t"
    "s_cbranch_scc1 .Lpub2%=\n\t"
    "s_mov_b32 s51, 0\n\t"
    "s_waitcnt vmcnt(0)\n\t"
    ".Lsw%=:\n\t"
    "ds_read_b128 v[24:27], v38 offset:16\n\t"
    "s_waitcnt lgkmcnt(0)\n\t"
    "v_min3_u32 v24, v24, v25, v26\n\t"
    "v_min_u32 v24, v24, v27\n\t"
    "s_nop 0\n\t"
    "v_readfirstlane_b32 s34, v24\n\t"
    "s_cmp_ge_i32 s34, 17\n\t"
    "s_cbranch_scc1 .Lsg%=\n\t"
    "s_sleep 2\n\t"
    "s_branch .Lsw%=\n\t"
    ".Lsg%=:\n\t"
    "v_bfe_u32 v24, v1, 2, 3\n\t"
    "v_lshrrev_b32 v25, 5, v1\n\t"
    "s_and_b32 s36, s24, 1\n\t"
    "v_lshlrev_b32 v24, 8, v24\n\t"
    "v_xor_b32 v25, s36, v25\n\t"
    "v_lshl_add_u32 v24, v25, 6, v24\n\t"
    "v_lshrrev_b32 v25, 3, v1\n\t"
    "v_and_b32 v25, 2, v25\n\t"
    "v_and_b32 v26, 3, v1\n\t"
    "v_xor_b32 v26, v25, v26\n\t"
    "s_lshr_b32 s36, s24, 1\n\t"
    "s_lshl_b32 s36, s36, 7\n\t"
    "v_lshl_add_u32 v24, v26, 4, v24\n\t"
    "s_add_u32 s37, s36, 0x400\n\t"
    "s_sub_u32 s36, 0x80, s36\n\t"
    "v_add_u32 v48, s37, v24\n\t"
    "v_add_u32 v49, s36, v24\n\t"
    "v_add_u32 v50, 0x1000, v48\n\t"
    "v_add_u32 v51, 0x1000, v49\n\t"
    "s_add_u32 s28, s10, s25\n\t"
    "s_addc_u32 s29, s11, 0\n\t"
    "s_sub_u32 s28, s28, 0x400\n\t"
    "s_subb_u32 s29, s29, 0\n\t"
    "s_add_u32 s30, s12, s25\n\t"
    "s_addc_u32 s31, s13, 0\n\t"
    "s_sub_u32 s30, s30, 0x400\n\t"
    "s_subb_u32 s31, s31, 0\n\t"
    "s_lshl_b32 s33, s40, 14\n\t"
    "s_add_u32 s37, s33, 0x2000\n\t"
    "v_add_u32 v32, s33, v29\n\t"
    "v_add_u32 v33, s33, v30\n\t"
    "v_add_u32 v34, s33, v31\n\t"
    "v_mov_b32 v35, 0xff800000\n\t"
    "v_mov_b32 v36, 0xff800000\n\t"
    "s_mov_b32 s35, 16\n\t"
    "s_mov_b32 s50, 0xff800000\n\t"
    "v_mov_b64 v[84:85], 0\n\t"
    "v_mov_b64 v[86:87], 0\n\t"
    "v_mov_b64 v[88:89], 0\n\t"
    "v_mov_b64 v[90:91], 0\n\t"
    "v_mov_b64 v[192:193], 0\n\t"
    "v_mov_b64 v[194:195], 0\n\t"
    "v_mov_b64 v[196:197], 0\n\t"
    "v_mov_b64 v[198:199], 0\n\t"
    "v_mov_b64 v[200:201], 0\n\t"
    "v_mov_b64 v[202:203], 0\n\t"
    "v_mov_b64 v[204:205], 0\n\t"
    "v_mov_b64 v[206:207], 0\n\t"
    "v_mov_b64 v[208:209], 0\n\t"
    "v_mov_b64 v[210:211], 0\n\t"
    "v_mov_b64 v[212:213], 0\n\t"
    "v_mov_b64 v[214:215], 0\n\t"
    "v_mov_b64 v[216:217], 0\n\t"
    "v_mov_b64 v[218:219], 0\n\t"
    "v_mov_b64 v[220:221], 0\n\t"
    "v_mov_b64 v[222:223], 0\n\t"
    "v_mov_b64 v[224:225], 0\n\t"
    "v_mov_b64 v[226:227], 0\n\t"
    "v_mov_b64 v[228:229], 0\n\t"
    "v_mov_b64 v[230:231], 0\n\t"
    "v_mov_b64 v[232:233], 0\n\t"
    "v_mov_b64 v[234:235], 0\n\t"
    "v_mov_b64 v[236:237], 0\n\t"
    "v_mov_b64 v[238:239], 0\n\t"
    "v_mov_b64 v[240:241], 0\n\t"
    "v_mov_b64 v[242:243], 0\n\t"
    "v_mov_b64 v[244:245], 0\n\t"
    "v_mov_b64 v[246:247], 0\n\t"
    "v_mov_b64 v[248:249], 0\n\t"
    "v_mov_b64 v[250:251], 0\n\t"
    "v_mov_b64 v[252:253], 0\n\t"
    "v_mov_b64 v[254:255], 0\n\t"
    ".Lsl%=:\n\t"
    "s_mov_b32 m0, s33\n\t"
    "s_nop 0\n\t"
    "global_load_lds_dwordx4 v2, s[28:29] offset:0\n\t"
    "global_load_lds_dwordx4 v4, s[28:29] offset:1024\n\t"
    "global_load_lds_dwordx4 v3, s[28:29] offset:2048\n\t"
    "global_load_lds_dwordx4 v5, s[28:29] offset:3072\n\t"
    "s_add_u32 m0, m0, 0x1000\n\t"
    "s_nop 0\n\t"
    "global_load_lds_dwordx4 v6, s[28:29] offset:0\n\t"
    "global_load_lds_dwordx4 v8, s[28:29] offset:1024\n\t"
    "global_load_lds_dwordx4 v7, s[28:29] offset:2048\n\t"
    "global_load_lds_dwordx4 v9, s[28:29] offset:3072\n\t"
    "s_add_u32 s28, s28, 8192\n\t"
    "s_addc_u32 s29, s29, 0\n\t"
    "s_mov_b32 m0, s37\n\t"
    "s_nop 0\n\t"
    "global_load_lds_dwordx4 v48, s[30:31] offset:0\n\t"
    "global_load_lds_dwordx4 v49, s[30:31] offset:1024\n\t"
    "global_load_lds_dwordx4 v48, s[30:31] offset:2048\n\t"
    "global_load_lds_dwordx4 v49, s[30:31] offset:3072\n\t"
    "s_add_u32 m0, m0, 0x1000\n\t"
    "s_nop 0\n\t"
    "global_load_lds_dwordx4 v50, s[30:31] offset:0\n\t"
    "global_load_lds_dwordx4 v51, s[30:31] offset:1024\n\t"
    "global_load_lds_dwordx4 v50, s[30:31] offset:2048\n\t"
    "global_load_lds_dwordx4 v51, s[30:31] offset:3072\n\t"
    "s_add_u32 s30, s30, 8192\n\t"
    "s_addc_u32 s31, s31, 0\n\t"
    "s_waitcnt vmcnt(0)\n\t"
    "ds_read_b128 v[128:131], v32 offset:0\n\t"
    "ds_read_b128 v[132:135], v32 offset:512\n\t"
    "ds_read_b128 v[136:139], v32 offset:1024\n\t"
    "ds_read_b128 v[140:143], v32 offset:1536\n\t"
    "ds_read_b128 v[144:147], v32 offset:4096\n\t"
    "ds_read_b128 v[148:151], v32 offset:4608\n\t"
    "ds_read_b128 v[152:155], v32 offset:5120\n\t"
    "ds_read_b128 v[156:159], v32 offset:5632\n\t"
    "s_waitcnt lgkmcnt(0)\n\t"
    "ds_read_b64_tr_b16 v[96:97], v33 offset:8192\n\t"
    "ds_read_b64_tr_b16 v[98:99], v33 offset:12288\n\t"
    "ds_read_b64_tr_b16 v[100:101], v34 offset:8192\n\t"
    "ds_read_b64_tr_b16 v[102:103], v34 offset:12288\n\t"
    "ds_read_b64_tr_b16 v[104:105], v33 offset:8704\n\t"
    "ds_read_b64_tr_b16 v[106:107], v33 offset:12800\n\t"
    "ds_read_b64_tr_b16 v[108:109], v34 offset:8704\n\t"
    "ds_read_b64_tr_b16 v[110:111], v34 offset:12800\n\t"
    "ds_read_b64_tr_b16 v[112:113], v33 offset:9216\n\t"
    "ds_read_b64_tr_b16 v[114:115], v33 offset:13312\n\t"
    "ds_read_b64_tr_b16 v[116:117], v34 offset:9216\n\t"
    "ds_read_b64_tr_b16 v[118:119], v34 offset:13312\n\t"
    "ds_read_b64_tr_b16 v[120:121], v33 offset:9728\n\t"
    "ds_read_b64_tr_b16 v[122:123], v33 offset:13824\n\t"
    "ds_read_b64_tr_b16 v[124:125], v34 offset:9728\n\t"
    "ds_read_b64_tr_b16 v[126:127], v34 offset:13824\n\t"
    "v_mfma_f32_16x16x32_bf16 v[64:67], v[128:131], v[160:163], 0\n\t"
    "v_mfma_f32_16x16x32_bf16 v[68:71], v[128:131], v[176:179], 0\n\t"
    "v_mfma_f32_16x16x32_bf16 v[64:67], v[132:135], v[164:167], v[64:67]\n\t"
    "v_mfma_f32_16x16x32_bf16 v[68:71], v[132:135], v[180:183], v[68:71]\n\t"
    "v_mfma_f32_16x16x32_bf16 v[64:67], v[136:139], v[168:171], v[64:67]\n\t"
    "v_mfma_f32_16x16x32_bf16 v[68:71], v[136:139], v[184:187], v[68:71]\n\t"
    "v_mfma_f32_16x16x32_bf16 v[64:67], v[140:143], v[172:175], v[64:67]\n\t"
    "v_mfma_f32_16x16x32_bf16 v[68:71], v[140:143], v[188:191], v[68:71]\n\t"
    "v_mfma_f32_16x16x32_bf16 v[72:75], v[144:147], v[160:163], 0\n\t"
    "v_mfma_f32_16x16x32_bf16 v[76:79], v[144:147], v[176:179], 0\n\t"
    "v_mfma_f32_16x16x32_bf16 v[72:75], v[148:151], v[164:167], v[72:75]\n\t"
    "v_mfma_f32_16x16x32_bf16 v[76:79], v[148:151], v[180:183], v[76:79]\n\t"
    "v_mfma_f32_16x16x32_bf16 v[72:75], v[152:155], v[168:171], v[72:75]\n\t"
    "v_mfma_f32_16x16x32_bf16 v[76:79], v[152:155], v[184:187], v[76:79]\n\t"
    "v_mfma_f32_16x16x32_bf16 v[72:75], v[156:159], v[172:175], v[72:75]\n\t"
    "v_mfma_f32_16x16x32_bf16 v[76:79], v[156:159], v[188:191], v[76:79]\n\t"
    "s_nop 7\n\t"
    "s_nop 7\n\t"
    "s_waitcnt lgkmcnt(0)\n\t"
    "v_max3_f32 v24, v64, v65, v66\n\t"
    "v_max3_f32 v25, v67, v72, v73\n\t"
    "v_max3_f32 v24, v24, v74, v75\n\t"
    "v_max_f32 v24, v24, v25\n\t"
    "v_mov_b32 v25, v24\n\t"
    "s_nop 1\n\t"
    "v_permlane16_swap_b32 v24, v25\n\t"
    "s_nop 1\n\t"
    "v_max_f32 v24, v24, v25\n\t"
    "v_mov_b32 v25, v24\n\t"
    "s_nop 1\n\t"
    "v_permlane32_swap_b32 v24, v25\n\t"
    "s_nop 1\n\t"
    "v_max3_f32 v24, v24, v25, v35\n\t"
    "v_cmp_eq_f32 vcc, s50, v24\n\t"
    "s_nop 1\n\t"
    "v_cndmask_b32 v25, v24, v23, vcc\n\t"
    "v_sub_f32 v58, v35, v25\n\t"
    "v_mov_b32 v35, v24\n\t"
    "v_exp_f32 v58, v58\n\t"
    "v_xor_b32 v56, 0x80000000, v25\n\t"
    "v_mov_b32 v57, v56\n\t"
    "v_mov_b32 v59, v58\n\t"
    "v_pk_add_f32 v[64:65], v[64:65], v[56:57]\n\t"
    "v_pk_add_f32 v[66:67], v[66:67], v[56:57]\n\t"
    "v_pk_add_f32 v[72:73], v[72:73], v[56:57]\n\t"
    "v_pk_add_f32 v[74:75], v[74:75], v[56:57]\n\t"
    "v_pk_mul_f32 v[84:85], v[84:85], v[58:59]\n\t"
    "v_pk_mul_f32 v[86:87], v[86:87], v[58:59]\n\t"
    "v_pk_mul_f32 v[192:193], v[192:193], v[58:59]\n\t"
    "v_pk_mul_f32 v[194:195], v[194:195], v[58:59]\n\t"
    "v_pk_mul_f32 v[200:201], v[200:201], v[58:59]\n\t"
    "v_pk_mul_f32 v[202:203], v[202:203], v[58:59]\n\t"
    "v_pk_mul_f32 v[208:209], v[208:209], v[58:59]\n\t"
    "v_pk_mul_f32 v[210:211], v[210:211], v[58:59]\n\t"
    "v_pk_mul_f32 v[216:217], v[216:217], v[58:59]\n\t"
    "v_pk_mul_f32 v[218:219], v[218:219], v[58:59]\n\t"
    "v_pk_mul_f32 v[224:225], v[224:225], v[58:59]\n\t"
    "v_pk_mul_f32 v[226:227], v[226:227], v[58:59]\n\t"
    "v_pk_mul_f32 v[232:233], v[232:233], v[58:59]\n\t"
    "v_pk_mul_f32 v[234:235], v[234:235], v[58:59]\n\t"
    "v_pk_mul_f32 v[240:241], v[240:241], v[58:59]\n\t"
    "v_pk_mul_f32 v[242:243], v[242:243], v[58:59]\n\t"
    "v_pk_mul_f32 v[248:249], v[248:249], v[58:59]\n\t"
    "v_pk_mul_f32 v[250:251], v[250:251], v[58:59]\n\t"
    "v_max3_f32 v24, v68, v69, v70\n\t"
    "v_max3_f32 v25, v71, v76, v77\n\t"
    "v_max3_f32 v24, v24, v78, v79\n\t"
    "v_max_f32 v24, v24, v25\n\t"
    "v_mov_b32 v25, v24\n\t"
    "s_nop 1\n\t"
    "v_permlane16_swap_b32 v24, v25\n\t"
    "s_nop 1\n\t"
    "v_max_f32 v24, v24, v25\n\t"
    "v_mov_b32 v25, v24\n\t"
    "s_nop 1\n\t"
    "v_permlane32_swap_b32 v24, v25\n\t"
    "s_nop 1\n\t"
    "v_max3_f32 v24, v24, v25, v36\n\t"
    "v_cmp_eq_f32 vcc, s50, v24\n\t"
    "s_nop 1\n\t"
    "v_cndmask_b32 v25, v24, v23, vcc\n\t"
    "v_sub_f32 v62, v36, v25\n\t"
    "v_mov_b32 v36, v24\n\t"
    "v_exp_f32 v62, v62\n\t"
    "v_xor_b32 v60, 0x80000000, v25\n\t"
    "v_mov_b32 v61, v60\n\t"
    "v_mov_b32 v63, v62\n\t"
    "v_pk_add_f32 v[68:69], v[68:69], v[60:61]\n\t"
    "v_pk_add_f32 v[70:71], v[70:71], v[60:61]\n\t"
    "v_pk_add_f32 v[76:77], v[76:77], v[60:61]\n\t"
    "v_pk_add_f32 v[78:79], v[78:79], v[60:61]\n\t"
    "v_pk_mul_f32 v[88:89], v[88:89], v[62:63]\n\t"
    "v_pk_mul_f32 v[90:91], v[90:91], v[62:63]\n\t"
    "v_pk_mul_f32 v[196:197], v[196:197], v[62:63]\n\t"
    "v_pk_mul_f32 v[198:199], v[198:199], v[62:63]\n\t"
    "v_pk_mul_f32 v[204:205], v[204:205], v[62:63]\n\t"
    "v_pk_mul_f32 v[206:207], v[206:207], v[62:63]\n\t"
    "v_pk_mul_f32 v[212:213], v[212:213], v[62:63]\n\t"
    "v_pk_mul_f32 v[214:215], v[214:215], v[62:63]\n\t"
    "v_pk_mul_f32 v[220:221], v[220:221], v[62:63]\n\t"
    "v_pk_mul_f32 v[222:223], v[222:223], v[62:63]\n\t"
    "v_pk_mul_f32 v[228:229], v[228:229], v[62:63]\n\t"
    "v_pk_mul_f32 v[230:231], v[230:231], v[62:63]\n\t"
    "v_pk_mul_f32 v[236:237], v[236:237], v[62:63]\n\t"
    "v_pk_mul_f32 v[238:239], v[238:239], v[62:63]\n\t"
    "v_pk_mul_f32 v[244:245], v[244:245], v[62:63]\n\t"
    "v_pk_mul_f32 v[246:247], v[246:247], v[62:63]\n\t"
    "v_pk_mul_f32 v[252:253], v[252:253], v[62:63]\n\t"
    "v_pk_mul_f32 v[254:255], v[254:255], v[62:63]\n\t"
    "v_exp_f32 v64, v64\n\t"
    "v_exp_f32 v65, v65\n\t"
    "v_exp_f32 v66, v66\n\t"
    "v_exp_f32 v67, v67\n\t"
    "v_exp_f32 v68, v68\n\t"
    "v_exp_f32 v69, v69\n\t"
    "v_exp_f32 v70, v70\n\t"
    "v_exp_f32 v71, v71\n\t"
    "v_exp_f32 v72, v72\n\t"
    "v_exp_f32 v73, v73\n\t"
    "v_exp_f32 v74, v74\n\t"
    "v_exp_f32 v75, v75\n\t"
    "v_exp_f32 v76, v76\n\t"
    "v_exp_f32 v77, v77\n\t"
    "v_exp_f32 v78, v78\n\t"
    "v_exp_f32 v79, v79\n\t"
    "s_nop 0\n\t"
    "v_cvt_pk_bf16_f32 v40, v64, v65\n\t"
    "v_cvt_pk_bf16_f32 v41, v66, v67\n\t"
    "v_cvt_pk_bf16_f32 v44, v68, v69\n\t"
    "v_cvt_pk_bf16_f32 v45, v70, v71\n\t"
    "v_cvt_pk_bf16_f32 v42, v72, v73\n\t"
    "v_cvt_pk_bf16_f32 v43, v74, v75\n\t"
    "v_cvt_pk_bf16_f32 v46, v76, v77\n\t"
    "v_cvt_pk_bf16_f32 v47, v78, v79\n\t"
    "s_nop 1\n\t"
    "v_mfma_f32_16x16x32_bf16 v[84:87], v[80:83], v[40:43], v[84:87]\n\t"
    "v_mfma_f32_16x16x32_bf16 v[88:91], v[80:83], v[44:47], v[88:91]\n\t"
    "v_mfma_f32_16x16x32_bf16 v[192:195], v[96:99], v[40:43], v[192:195]\n\t"
    "v_mfma_f32_16x16x32_bf16 v[196:199], v[96:99], v[44:47], v[196:199]\n\t"
    "v_mfma_f32_16x16x32_bf16 v[200:203], v[100:103], v[40:43], v[200:203]\n\t"
    "v_mfma_f32_16x16x32_bf16 v[204:207], v[100:103], v[44:47], v[204:207]\n\t"
    "v_mfma_f32_16x16x32_bf16 v[208:211], v[104:107], v[40:43], v[208:211]\n\t"
    "v_mfma_f32_16x16x32_bf16 v[212:215], v[104:107], v[44:47], v[212:215]\n\t"
    "v_mfma_f32_16x16x32_bf16 v[216:219], v[108:111], v[40:43], v[216:219]\n\t"
    "v_mfma_f32_16x16x32_bf16 v[220:223], v[108:111], v[44:47], v[220:223]\n\t"
    "v_mfma_f32_16x16x32_bf16 v[224:227], v[112:115], v[40:43], v[224:227]\n\t"
    "v_mfma_f32_16x16x32_bf16 v[228:231], v[112:115], v[44:47], v[228:231]\n\t"
    "v_mfma_f32_16x16x32_bf16 v[232:235], v[116:119], v[40:43], v[232:235]\n\t"
    "v_mfma_f32_16x16x32_bf16 v[236:239], v[116:119], v[44:47], v[236:239]\n\t"
    "v_mfma_f32_16x16x32_bf16 v[240:243], v[120:123], v[40:43], v[240:243]\n\t"
    "v_mfma_f32_16x16x32_bf16 v[244:247], v[120:123], v[44:47], v[244:247]\n\t"
    "v_mfma_f32_16x16x32_bf16 v[248:251], v[124:127], v[40:43], v[248:251]\n\t"
    "v_mfma_f32_16x16x32_bf16 v[252:255], v[124:127], v[44:47], v[252:255]\n\t"
    "s_nop 7\n\t"
    "s_nop 7\n\t"
    "s_sub_u32 s35, s35, 1\n\t"
    "s_cmp_lg_u32 s35, 0\n\t"
    "s_cbranch_scc1 .Lsl%=\n\t"
    "v_xor_b32 v26, s24, v1\n\t"
    "v_and_b32 v26, 3, v26\n\t"
    "v_lshlrev_b32 v26, 7, v26\n\t"
    "v_add_u32 v26, 33792, v26\n\t"
    "s_branch .Lpub%=\n\t"
    :
    : [karg] "s"(__builtin_amdgcn_kernarg_segment_ptr()), [bid] "s"(blockIdx.x), "{v0}"(threadIdx.x), "s"((unsigned)(size_t)lds)
    : "memory", "scc", "vcc", "v1", "v2", "v3", "v4", "v5", "v6", "v7", "v8", "v9", "v10", "v11", "v12", "v13", "v14", "v15", "v16", "v17", "v18", "v19", "v20", "v21", "v22", "v23", "v24", "v25", "v26", "v27", "v28", "v29", "v30", "v31", "v32", "v33", "v34", "v35", "v36", "v37", "v38", "v39", "v40", "v41", "v42", "v43", "v44", "v45", "v46", "v47", "v48", "v49", "v50", "v51", "v52", "v53", "v54", "v55", "v56", "v57", "v58", "v59", "v60", "v61", "v62", "v63", "v64", "v65", "v66", "v67", "v68", "v69", "v70", "v71", "v72", "v73", "v74", "v75", "v76", "v77", "v78", "v79", "v80", "v81", "v82", "v83", "v84", "v85", "v86", "v87", "v88", "v89", "v90", "v91", "v92", "v93", "v94", "v95", "v96", "v97", "v98", "v99", "v100", "v101", "v102", "v103", "v104", "v105", "v106", "v107", "v108", "v109", "v110", "v111", "v112", "v113", "v114", "v115", "v116", "v117", "v118", "v119", "v120", "v121", "v122", "v123", "v124", "v125", "v126", "v127", "v128", "v129", "v130", "v131", "v132", "v133", "v134", "v135", "v136", "v137", "v138", "v139", "v140", "v141", "v142", "v143", "v144", "v145", "v146", "v147", "v148", "v149", "v150", "v151", "v152", "v153", "v154", "v155", "v156", "v157", "v158", "v159", "v160", "v161", "v162", "v163", "v164", "v165", "v166", "v167", "v168", "v169", "v170", "v171", "v172", "v173", "v174", "v175", "v176", "v177", "v178", "v179", "v180", "v181", "v182", "v183", "v184", "v185", "v186", "v187", "v188", "v189", "v190", "v191", "v192", "v193", "v194", "v195", "v196", "v197", "v198", "v199", "v200", "v201", "v202", "v203", "v204", "v205", "v206", "v207", "v208", "v209", "v210", "v211", "v212", "v213", "v214", "v215", "v216", "v217", "v218", "v219", "v220", "v221", "v222", "v223", "v224", "v225", "v226", "v227", "v228", "v229", "v230", "v231", "v232", "v233", "v234", "v235", "v236", "v237", "v238", "v239", "v240", "v241", "v242", "v243", "v244", "v245", "v246", "v247", "v248", "v249", "v250", "v251", "v252", "v253", "v254", "v255", "s8", "s9", "s10", "s11", "s12", "s13", "s14", "s15", "s16", "s17", "s18", "s19", "s20", "s21", "s22", "s23", "s24", "s25", "s26", "s27", "s28", "s29", "s30", "s31", "s33", "s34", "s35", "s36", "s37", "s38", "s39", "s40", "s41", "s42", "s43", "s44", "s45", "s46", "s47", "s48", "s49", "s50", "s51", "s52", "s53", "s54", "s55", "s56", "s57", "s58", "s59", "s60", "s61", "s62", "s63", "s64", "s65", "s66", "s67", "s68", "s69", "s70", "s71", "s72", "s73", "s74", "s75", "s76", "s77", "s78", "s79", "s80", "s81", "s82", "s83", "s84", "s85", "s86", "s87", "s88", "s89");
}
extern "C" void kernel_launch(void* const* d_in, const int* in_sizes, int n_in, void* d_out, int out_size, void* d_ws, size_t ws_size, hipStream_t stream) {
  (void)in_sizes; (void)n_in; (void)out_size; (void)ws_size;
  gqa_fwd<<<256, 384, 0, stream>>>((const char*)d_in[0], (const char*)d_in[1], (const char*)d_in[2], (char*)d_out, (char*)d_ws);
}
```
